```python
import torch

def _softmax(x, axis=-1):
    return torch.softmax(x, dim=axis)
def _gelu(x):
    return torch.nn.functional.gelu(x, approximate="tanh")
def _silu(x):
    return torch.nn.functional.silu(x)
def _sigmoid(x):
    return torch.sigmoid(x)
def _layernorm(x, axis=-1, eps=1e-5):
    m = x.mean(dim=axis, keepdim=True)
    v = x.var(dim=axis, keepdim=True, unbiased=False)
    return (x - m) / torch.sqrt(v + eps)
def _scatter_add(tgt, idx, src, axis=0):
    return tgt.clone().index_add_(axis, idx, src)
def _segment_sum(src, seg, n):
    out = torch.zeros((n,) + src.shape[1:], dtype=src.dtype)
    return out.index_add_(0, seg, src)
def _pad(x, pw):
    flat = tuple(v for pair in reversed(pw) for v in pair)
    return torch.nn.functional.pad(x, flat)

kernel_name = 'recurrent_scan__compressed_kv_cla__dense_activation_relu__decode'

def setup_inputs():

    _w = (lambda a: (lambda t: t.to(torch.bfloat16) if t.is_floating_point() else t)(torch.as_tensor(a)))
    def _A_setup():
        B, H, Sq, Sk, D, M = 1, 32, 1, 16384, 128, 640
        Dc = 4 * D
        return {
            "x": _w((lambda *s: torch.randn(*s, dtype=torch.bfloat16) * 0.05)(B, Sq, M)),
            "c_kv": _w((lambda *s: torch.randn(*s, dtype=torch.bfloat16) * 0.8)(B, Sk, Dc)),
            "w_q": _w((lambda *s: torch.randn(*s, dtype=torch.bfloat16) * 1.6)(M, H, D) * (M ** -0.5)),
            "w_uk": _w((lambda *s: torch.randn(*s, dtype=torch.bfloat16) * 1.6)(Dc, H, D) * (Dc ** -0.5)),
            "w_uv": _w((lambda *s: torch.randn(*s, dtype=torch.bfloat16) * 0.8)(Dc, H, D) * (Dc ** -0.5)),
            "w_o": _w((lambda *s: torch.randn(*s, dtype=torch.bfloat16) * 0.8)(H, D, M) * ((H * D) ** -0.5)),
        }
    _A_in = _A_setup()

    def _B_setup():
        B, S, D, Dff = 1, 1, 640, 1728
        return {
            "x": _w((lambda *s: torch.randn(*s, dtype=torch.bfloat16) * 0.05)(B, S, D)),
            "w1": _w((lambda *s: torch.randn(*s, dtype=torch.bfloat16) * 0.2)(D, Dff)),
            "w2": _w((lambda *s: torch.randn(*s, dtype=torch.bfloat16) * 0.2)(Dff, D)),
        }
    _B_in = _B_setup()

    return {
        "A_x": _A_in["x"],
        "A_c_kv": _A_in["c_kv"],
        "A_w_q": _A_in["w_q"],
        "A_w_uk": _A_in["w_uk"],
        "A_w_uv": _A_in["w_uv"],
        "A_w_o": _A_in["w_o"],
        "B_x": _B_in["x"],
        "B_w1": _B_in["w1"],
        "B_w2": _B_in["w2"],
    }

def reference(A_x, A_c_kv, A_w_q, A_w_uk, A_w_uv, A_w_o, B_x, B_w1, B_w2):
    def _A_ref(x, c_kv, w_q, w_uk, w_uv, w_o):
        B, Sq, M = x.shape[0], x.shape[1], x.shape[2]
        Sk = c_kv.shape[1]
        H, D = w_q.shape[1], w_q.shape[2]
        q = torch.einsum("bqm,mhd->bhqd", x, w_q)
        k = torch.einsum("bkc,chd->bhkd", c_kv, w_uk)
        v = torch.einsum("bkc,chd->bhkd", c_kv, w_uv)
        scores = torch.einsum("bhqd,bhkd->bhqk", q, k) * (D ** -0.5)
        off = Sk - Sq
        qi = torch.arange(Sq)[:, None]
        kj = torch.arange(Sk)[None, :]
        cmask = kj <= (qi + off)
        scores = torch.where(cmask[None, None], scores, -1e9)
        p = _softmax(scores, axis=-1)
        ctx = torch.einsum("bhqk,bhkd->bhqd", p, v)
        return torch.einsum("bhqd,hdm->bqm", ctx, w_o)

    def _B_ref(x, w1, w2):
        h = torch.einsum("bsd,df->bsf", x, w1)
        zero = torch.zeros_like(h)
        h = torch.maximum(h, zero)
        return torch.einsum("bsf,fd->bsd", h, w2)

    x = A_x
    xn = _layernorm(x, axis=-1)
    s = _A_ref(xn, A_c_kv, A_w_q, A_w_uk, A_w_uv, A_w_o)
    g = _silu(_B_ref(xn, B_w1, B_w2))
    return x + s * g
```

```cpp
#include <hip/hip_runtime.h>
#include <hip/hip_bf16.h>
#include <stdint.h>

typedef unsigned short u16;
__device__ __forceinline__ float bf2f(u16 b) { return __uint_as_float(((unsigned)b) << 16); }
__device__ __forceinline__ u16 f2bf(float f) { unsigned u = __float_as_uint(f); u += 0x7FFFu + ((u >> 16) & 1u); return (u16)(u >> 16); }
__device__ __forceinline__ float rbf(float f) { return bf2f(f2bf(f)); }

constexpr int M = 640, H = 32, D = 128, SK = 16384, DC = 512, DFF = 1728;

__device__ void layernorm_to_lds(const u16* x, float* xn, float* red) {
  int tid = threadIdx.x;
  float s = 0.f;
  for (int i = tid; i < M; i += blockDim.x) s += bf2f(x[i]);
  for (int o = 32; o; o >>= 1) s += __shfl_xor(s, o);
  if ((tid & 63) == 0) red[tid >> 6] = s;
  __syncthreads();
  float tot = 0.f; for (int w = 0; w < (int)blockDim.x / 64; ++w) tot += red[w];
  float mean = tot / M;
  __syncthreads();
  float v = 0.f;
  for (int i = tid; i < M; i += blockDim.x) { float d = bf2f(x[i]) - mean; v += d * d; }
  for (int o = 32; o; o >>= 1) v += __shfl_xor(v, o);
  if ((tid & 63) == 0) red[tid >> 6] = v;
  __syncthreads();
  float vt = 0.f; for (int w = 0; w < (int)blockDim.x / 64; ++w) vt += red[w];
  float var = rbf(vt / M);
  float mb = rbf(mean);
  float sd = rbf(sqrtf(rbf(var + 1e-5f)));
  for (int i = tid; i < M; i += blockDim.x) xn[i] = rbf(rbf(bf2f(x[i]) - mb) / sd);
  __syncthreads();
}

__global__ void k1(const u16* x, const u16* wq, const u16* w1, float* q, float* hmid) {
  __shared__ float xn[M]; __shared__ float red[8];
  layernorm_to_lds(x, xn, red);
  int col = blockIdx.x * 256 + threadIdx.x;
  if (col < 4096) {
    float a = 0.f;
    for (int m = 0; m < M; ++m) a += xn[m] * bf2f(wq[m * 4096 + col]);
    q[col] = rbf(a);
  } else if (col - 4096 < DFF) {
    int f = col - 4096; float a = 0.f;
    for (int m = 0; m < M; ++m) a += xn[m] * bf2f(w1[m * DFF + f]);
    hmid[f] = fmaxf(rbf(a), 0.f);
  }
}
__global__ void k2(const float* q, const u16* wuk, const float* hmid, const u16* w2, float* qa, float* y) {
  int idx = blockIdx.x * 256 + threadIdx.x;
  if (idx < H * DC) {
    int h = idx / DC, c = idx % DC; float a = 0.f;
    for (int d = 0; d < D; ++d) a += q[h * D + d] * bf2f(wuk[(c * H + h) * D + d]);
    qa[idx] = a;
  } else if (idx - H * DC < M) {
    int m = idx - H * DC; float a = 0.f;
    for (int f = 0; f < DFF; ++f) a += hmid[f] * bf2f(w2[f * M + m]);
    y[m] = rbf(a);
  }
}
__global__ void k3(const float* qa, const u16* ckv, float* S) {
  int k = blockIdx.x * 8 + (threadIdx.x >> 5), h = threadIdx.x & 31;
  float a = 0.f;
  for (int c = 0; c < DC; ++c) a += qa[h * DC + c] * bf2f(ckv[k * DC + c]);
  S[h * SK + k] = a * 0.08838834764831845f;
}
__global__ void k4(float* S, float* linv) {
  __shared__ float red[16];
  int h = blockIdx.x, tid = threadIdx.x;
  float mx = -1e30f;
  for (int k = tid; k < SK; k += blockDim.x) mx = fmaxf(mx, S[h * SK + k]);
  for (int o = 32; o; o >>= 1) mx = fmaxf(mx, __shfl_xor(mx, o));
  if ((tid & 63) == 0) red[tid >> 6] = mx;
  __syncthreads();
  for (int w = 0; w < (int)blockDim.x / 64; ++w) mx = fmaxf(mx, red[w]);
  __syncthreads();
  float s = 0.f;
  for (int k = tid; k < SK; k += blockDim.x) { float p = __expf(S[h * SK + k] - mx); S[h * SK + k] = p; s += p; }
  for (int o = 32; o; o >>= 1) s += __shfl_xor(s, o);
  if ((tid & 63) == 0) red[tid >> 6] = s;
  __syncthreads();
  if (tid == 0) { float t = 0.f; for (int w = 0; w < (int)blockDim.x / 64; ++w) t += red[w]; linv[h] = 1.f / t; }
}
__global__ void k5(const float* S, const u16* ckv, float* Op) {
  int h = blockIdx.x, ch = blockIdx.y, c = threadIdx.x;
  float a = 0.f;
  for (int k = ch * 1024; k < ch * 1024 + 1024; ++k) a += S[h * SK + k] * bf2f(ckv[k * DC + c]);
  Op[(ch * H + h) * DC + c] = a;
}
__global__ void k6(const float* Op, const float* linv, const u16* wuv, float* ctx) {
  __shared__ float o[DC];
  int h = blockIdx.x, tid = threadIdx.x;
  for (int c = tid; c < DC; c += blockDim.x) { float a = 0.f; for (int ch = 0; ch < 16; ++ch) a += Op[(ch * H + h) * DC + c]; o[c] = a * linv[h]; }
  __syncthreads();
  float a = 0.f;
  for (int c = 0; c < DC; ++c) a += o[c] * bf2f(wuv[(c * H + h) * D + tid]);
  ctx[h * D + tid] = rbf(a);
}
__global__ void k7(const float* ctx, const u16* wo, const float* y, const u16* x, u16* out) {
  __shared__ float red[8][64];
  int m = blockIdx.x * 64 + (threadIdx.x & 63), w = threadIdx.x >> 6;
  float a = 0.f;
  for (int i = w * 512; i < w * 512 + 512; ++i) a += ctx[i] * bf2f(wo[i * M + m]);
  red[w][threadIdx.x & 63] = a;
  __syncthreads();
  if (w == 0) {
    float s = 0.f; for (int i = 0; i < 8; ++i) s += red[i][threadIdx.x];
    s = rbf(s);
    float yy = y[m]; float g = rbf(yy / (1.f + __expf(-yy)));
    out[m] = f2bf(bf2f(x[m]) + rbf(s * g));
  }
}

extern "C" void kernel_launch(void* const* d_in, const int* in_sizes, int n_in, void* d_out, int out_size, void* d_ws, size_t ws_size, hipStream_t stream) {
  const u16* x = (const u16*)d_in[0]; const u16* ckv = (const u16*)d_in[1]; const u16* wq = (const u16*)d_in[2];
  const u16* wuk = (const u16*)d_in[3]; const u16* wuv = (const u16*)d_in[4]; const u16* wo = (const u16*)d_in[5];
  const u16* w1 = (const u16*)d_in[7]; const u16* w2 = (const u16*)d_in[8];
  float* ws = (float*)d_ws;
  float* q = ws; float* hmid = ws + 4096; float* qa = ws + 8192; float* y = qa + H * DC; float* linv = y + 1024; float* ctx = linv + 64;
  float* S = ws + 65536; float* Op = S + H * SK;
  k1<<<(4096 + DFF + 255) / 256, 256, 0, stream>>>(x, wq, w1, q, hmid);
  k2<<<(H * DC + M + 255) / 256, 256, 0, stream>>>(q, wuk, hmid, w2, qa, y);
  k3<<<SK / 8, 256, 0, stream>>>(qa, ckv, S);
  k4<<<H, 1024, 0, stream>>>(S, linv);
  k5<<<dim3(H, 16), 512, 0, stream>>>(S, ckv, Op);
  k6<<<H, 128, 0, stream>>>(Op, linv, wuv, ctx);
  k7<<<M / 64, 512, 0, stream>>>(ctx, wo, y, x, (u16*)d_out);
}
```

```cpp
#include <hip/hip_runtime.h>
#include <hip/hip_bf16.h>
#include <stdint.h>

typedef unsigned short u16;
typedef unsigned int u32;
typedef short v4s __attribute__((ext_vector_type(4)));
typedef __bf16 v8bf __attribute__((ext_vector_type(8)));
typedef __bf16 v2bf __attribute__((ext_vector_type(2)));
typedef float v4f __attribute__((ext_vector_type(4)));
typedef float v2f __attribute__((ext_vector_type(2)));
typedef u32 v4u __attribute__((ext_vector_type(4)));
typedef u32 v2u __attribute__((ext_vector_type(2)));
#define LAS __attribute__((address_space(3)))
typedef __amdgpu_buffer_rsrc_t rsrc_t;

#define KV_WIN 24
#define KV_DELAY 12
#define W_AUX 0
#define STR2(x) #x
#define STR(x) STR2(x)

constexpr u32 WS_EPOCH = 0;
constexpr u32 WS_Q1 = 4096;
constexpr u32 WS_QA = WS_Q1 + 65536;
constexpr u32 WS_ML = WS_QA + 65536;
constexpr u32 WS_CX = WS_ML + 32768;
constexpr u32 WS_F = WS_CX + 131072;
constexpr u32 WS_OP = WS_F + 80 * 16384;
constexpr u32 WS_END = WS_OP + 256 * 16384;
constexpr u32 WS_STAMP = WS_END;

constexpr u32 RS = 1040;
constexpr u32 L_KV = 0;
constexpr u32 L_QA = 128 * RS;
constexpr u32 L_P = L_QA + 17408;
constexpr u32 L_MX = L_P + 4096;
constexpr u32 L_LS = L_MX + 512;
constexpr u32 L_QR = L_LS + 512;
constexpr u32 L_HR = L_QR + 640;
constexpr u32 L_OA = L_HR + 192;
constexpr u32 L_CNT = L_OA + 16 * 144;
constexpr u32 L_TOTAL = L_CNT + 64;
constexpr u32 UVS = 272;
static_assert(64 * UVS <= 17408, "uv image");
static_assert(L_TOTAL <= 163840, "lds");

constexpr u32 SENT = 0xFFFFFFFFu;
constexpr u32 SPIN_CAP = 1u << 18;

__device__ __forceinline__ float u2f(u32 u) { return __uint_as_float(u); }
__device__ __forceinline__ u32 f2u(float f) { return __float_as_uint(f); }
__device__ __forceinline__ float lo2f(u32 u) { return __uint_as_float(u << 16); }
__device__ __forceinline__ float hi2f(u32 u) { return __uint_as_float(u & 0xFFFF0000u); }
__device__ __forceinline__ u32 pk2(float a, float b) { v2f f = {a, b}; v2bf c = __builtin_convertvector(f, v2bf); return *(u32*)&c; }
__device__ __forceinline__ float rbf(float a) { return lo2f(pk2(a, a)); }
__device__ __forceinline__ u32 canon(u32 u) { return u < 0xFFFFFFFEu ? u : 0xFFFFFFFEu; }
__device__ __forceinline__ u32 max3u(u32 a, u32 b, u32 c) { u32 m = a > b ? a : b; return m > c ? m : c; }
__device__ __forceinline__ u32 max4u(v4u v) { return max3u(max3u(v[0], v[1], v[2]), v[3], v[3]); }
template <int CTRL> __device__ __forceinline__ float dpp(float v) { return u2f(__builtin_amdgcn_mov_dpp(f2u(v), CTRL, 0xF, 0xF, true)); }
constexpr int DPP_X1 = 0xB1, DPP_X2 = 0x4E, DPP_ROR4 = 0x124, DPP_ROR8 = 0x128, DPP_HMIR = 0x141;
__device__ __forceinline__ float sum16(float v) { v += dpp<DPP_X1>(v); v += dpp<DPP_X2>(v); v += dpp<DPP_ROR4>(v); v += dpp<DPP_ROR8>(v); return v; }
__device__ __forceinline__ float max16(float v) { v = fmaxf(v, dpp<DPP_X1>(v)); v = fmaxf(v, dpp<DPP_X2>(v)); v = fmaxf(v, dpp<DPP_ROR4>(v)); v = fmaxf(v, dpp<DPP_ROR8>(v)); return v; }
__device__ __forceinline__ float tr16(float a, float b) { auto r = __builtin_amdgcn_permlane16_swap(f2u(a), f2u(b), false, false); u32 x = r[0], y = r[1]; return u2f(x) + u2f(y); }
__device__ __forceinline__ float tr32(float a, float b) { auto r = __builtin_amdgcn_permlane32_swap(f2u(a), f2u(b), false, false); u32 x = r[0], y = r[1]; return u2f(x) + u2f(y); }
__device__ __forceinline__ float mx16(float a) { auto r = __builtin_amdgcn_permlane16_swap(f2u(a), f2u(a), false, false); u32 x = r[0], y = r[1]; return fmaxf(u2f(x), u2f(y)); }
__device__ __forceinline__ float mx32(float a) { auto r = __builtin_amdgcn_permlane32_swap(f2u(a), f2u(a), false, false); u32 x = r[0], y = r[1]; return fmaxf(u2f(x), u2f(y)); }
__device__ __forceinline__ float sum64(float v) { v = sum16(v); v = tr16(v, v); return tr32(v, v); }
__device__ __forceinline__ float max64(float v) { v = max16(v); v = mx16(v); return mx32(v); }
__device__ __forceinline__ float dot2(u32 a, u32 b, float c) { return __builtin_amdgcn_fdot2_f32_bf16(*(v2bf*)&a, *(v2bf*)&b, c, false); }
__device__ __forceinline__ v4f mfma(v4u a, v4u b, v4f c) { return __builtin_amdgcn_mfma_f32_16x16x32_bf16(*(v8bf*)&a, *(v8bf*)&b, c, 0, 0, 0); }
__device__ __forceinline__ rsrc_t mk(const void* p, u32 n) { return __builtin_amdgcn_make_buffer_rsrc((void*)p, 0, n, 0x00020000); }
__device__ __forceinline__ v4u ldw(rsrc_t r, u32 v, u32 s) { return __builtin_amdgcn_raw_buffer_load_b128(r, v, s, W_AUX); }
__device__ __forceinline__ v4u ldc(rsrc_t r, u32 v, u32 s) { return __builtin_amdgcn_raw_buffer_load_b128(r, v, s, 16); }
__device__ __forceinline__ v2u ldc2(rsrc_t r, u32 v, u32 s) { return __builtin_amdgcn_raw_buffer_load_b64(r, v, s, 16); }
__device__ __forceinline__ void stc(v4u d, rsrc_t r, u32 v, u32 s) { __builtin_amdgcn_raw_buffer_store_b128(d, r, v, s, 16); }
__device__ __forceinline__ void stc2(v2u d, rsrc_t r, u32 v, u32 s) { __builtin_amdgcn_raw_buffer_store_b64(d, r, v, s, 16); }
__device__ __forceinline__ float ldx(rsrc_t r, u32 elem) { return lo2f((u32)__builtin_amdgcn_raw_buffer_load_b16(r, elem * 2, 0, 0)); }
#define BAR() asm volatile("s_waitcnt lgkmcnt(0)\n\ts_barrier" ::: "memory")
#define CLOB() asm volatile("" ::: "memory")
#define VM0() asm volatile("s_waitcnt vmcnt(0)" ::: "memory")
template <typename T> __device__ __forceinline__ LAS T* lds(u32 off) { return (LAS T*)(uintptr_t)off; }
__device__ __forceinline__ void fma8(float* a, v4u w, float x) {
#pragma unroll
  for (int e = 0; e < 4; ++e) { a[2 * e] = fmaf(lo2f(w[e]), x, a[2 * e]); a[2 * e + 1] = fmaf(hi2f(w[e]), x, a[2 * e + 1]); }
}
__device__ __forceinline__ void red8(const float* a, float& o0, float& o1) {
  float t0 = tr32(a[0], a[4]), t1 = tr32(a[1], a[5]), t2 = tr32(a[2], a[6]), t3 = tr32(a[3], a[7]);
  o0 = tr16(t0, t2); o1 = tr16(t1, t3);
}

#define STAMP(k) do {} while (0)

__global__ void __launch_bounds__(512, 2)
mla_ffn_kernel(const u16* x, const u16* ckv, const u16* wq, const u16* wuk,
               const u16* wuv, const u16* wo, const u16* w1, const u16* w2,
               u16* out, char* ws) {
  extern __shared__ __attribute__((aligned(16))) char smem_[];
  STAMP(0);
  const u32 bi = blockIdx.x, xc = bi & 7, r = bi >> 3;
  const u32 type = r & 1;
  const u32 chunk = xc * 16 + (r >> 1);
  const u32 h = (1 - type) * 16 + xc * 2 + (r >> 4), j = (r >> 1) & 7;
  const u32 w = __builtin_amdgcn_readfirstlane(threadIdx.x >> 6), l = threadIdx.x & 63;
  const u32 l15 = l & 15, g = l >> 4;
  const rsrc_t rws = mk(ws, WS_END);
  const rsrc_t rx = mk(x, 1280);
  STAMP(14);
  const u32 tag = __builtin_amdgcn_raw_buffer_load_b32(rws, 0, WS_EPOCH, 16) + 1u;
  float mean_b = 0.f, sd_b = 1.f;
  if (threadIdx.x == 0) { *lds<v2u>(L_CNT) = (v2u){0u, 0u}; }

  if (w < 5) {
    u32 xs[5];
#pragma unroll
    for (int k = 0; k < 5; ++k) xs[k] = __builtin_amdgcn_raw_buffer_load_b32(rx, l * 4 + k * 256, 0, 0);
    const u32 m0 = 320 * (j & 1) + 64 * w + (l >> 2);
    float xr[4];
#pragma unroll
    for (int t = 0; t < 4; ++t) xr[t] = ldx(rx, m0 + 16 * t);
    const rsrc_t rq = mk(wq, 640 * 4096 * 2);
    v4u qv[4];
#pragma unroll
    for (int t = 0; t < 4; ++t) qv[t] = ldw(rq, m0 * 8192 + (l & 3) * 16 + t * 16 * 8192, h * 256 + (j >> 1) * 64);
    const rsrc_t rk = mk(wuk, w < 4 ? 512 * 4096 * 2 : 0);
    v4u kv[4];
#pragma unroll
    for (int t = 0; t < 4; ++t) kv[t] = ldw(rk, (4 * g + t) * 8192 + l15 * 16, (64 * j + 16 * w) * 8192 + h * 256);
    BAR();
    float s = 0.f;
#pragma unroll
    for (int k = 0; k < 5; ++k) s += lo2f(xs[k]) + hi2f(xs[k]);
    const float mean = sum64(s) * (1.f / 640.f);
    float v = 0.f;
#pragma unroll
    for (int k = 0; k < 5; ++k) { float a = lo2f(xs[k]) - mean, b = hi2f(xs[k]) - mean; v = fmaf(a, a, v); v = fmaf(b, b, v); }
    v = rbf(sum64(v) * (1.f / 640.f));
    mean_b = rbf(mean);
    sd_b = rbf(sqrtf(rbf(v + 1e-5f)));
    STAMP(1);
    float a[8] = {0.f, 0.f, 0.f, 0.f, 0.f, 0.f, 0.f, 0.f};
#pragma unroll
    for (int t = 0; t < 4; ++t) fma8(a, qv[t], rbf(rbf(xr[t] - mean_b) / sd_b));
    float o0, o1;
    red8(a, o0, o1);
    o0 += dpp<DPP_ROR4>(o0); o1 += dpp<DPP_ROR4>(o1);
    o0 += dpp<DPP_ROR8>(o0); o1 += dpp<DPP_ROR8>(o1);
    if (l15 < 4) *lds<v2f>(L_QR + w * 128 + (l15 * 8 + g * 2) * 4) = (v2f){o0, o1};
    asm volatile("s_waitcnt lgkmcnt(0)" ::: "memory");
    u32 arrived = 0;
    if (l == 0) arrived = __hip_atomic_fetch_add(lds<u32>(L_CNT), 1u, __ATOMIC_RELAXED, __HIP_MEMORY_SCOPE_WORKGROUP);
    arrived = __builtin_amdgcn_readfirstlane(arrived);
    if (arrived == 6) {
      CLOB();
      if (l < 16) {
        v2f sacc = *lds<v2f>(L_QR + l * 8);
#pragma unroll
        for (int u = 1; u < 5; ++u) sacc += *lds<v2f>(L_QR + u * 128 + l * 8);
        stc((v4u){f2u(sacc[0]), tag, f2u(sacc[1]), tag}, rws, l * 16, WS_Q1 + h * 2048 + (j & 1) * 1024 + (j >> 1) * 256);
      }
    }
    STAMP(2);
    if (w < 4) {
      v4u p0, p1;
      for (u32 it = 0; it < SPIN_CAP; ++it) {
        CLOB();
        p0 = ldc(rws, (l15 * 4 + g) * 16, WS_Q1 + h * 2048);
        p1 = ldc(rws, (l15 * 4 + g) * 16 + 1024, WS_Q1 + h * 2048);
        const bool ok = ((p0[1] ^ tag) | (p0[3] ^ tag) | (p1[1] ^ tag) | (p1[3] ^ tag)) == 0;
        if (__all(ok)) break;
      }
      STAMP(3);
      const u32 qp = pk2(u2f(p0[0]) + u2f(p1[0]), u2f(p0[2]) + u2f(p1[2]));
      auto s16 = __builtin_amdgcn_permlane16_swap(qp, qp, false, false);
      const u32 e02 = s16[0], e13 = s16[1];
      auto sa = __builtin_amdgcn_permlane32_swap(e02, e02, false, false);
      auto sb = __builtin_amdgcn_permlane32_swap(e13, e13, false, false);
      const u32 q0 = sa[0], q2 = sa[1], q1 = sb[0], q3 = sb[1];
      float d[4];
#pragma unroll
      for (int t = 0; t < 4; ++t) {
        float acc = dot2(kv[t][0], q0, 0.f);
        acc = dot2(kv[t][1], q1, acc); acc = dot2(kv[t][2], q2, acc); acc = dot2(kv[t][3], q3, acc);
        d[t] = sum16(acc) * (0.08838834764831845f * 1.4426950408889634f);
      }
      if (l15 == 0) stc((v4u){pk2(d[0], d[1]), tag, pk2(d[2], d[3]), tag}, rws, g * 16, WS_QA + h * 2048 + j * 256 + w * 64);
      STAMP(4);
    }
  } else if (w < 7) {
    const v4u sv = {SENT, SENT, SENT, SENT};
    const u32 t0 = (w - 5) * 64 + l;
#pragma unroll
    for (int k = 0; k < 8; ++k) stc(sv, rws, t0 * 16 + k * 2048, WS_OP + (h * 8 + j) * 16384);
#pragma unroll
    for (int k = 0; k < 2; ++k) stc(sv, rws, t0 * 16 + k * 2048, WS_F + bi * 5120);
    if (w == 5) stc(sv, rws, l * 16 + 4096, WS_F + bi * 5120);
    else if (l < 32) stc(sv, rws, (l >> 2) * 512 + (l & 3) * 16, WS_CX + h * 4096 + j * 64);
    else if (l < 40) stc(sv, rws, (l - 32) * 16, WS_ML + h * 1024 + j * 128);
    BAR();
    VM0();
    u32 arrived = 0;
    if (l == 0) arrived = __hip_atomic_fetch_add(lds<u32>(L_CNT), 1u, __ATOMIC_RELAXED, __HIP_MEMORY_SCOPE_WORKGROUP);
    arrived = __builtin_amdgcn_readfirstlane(arrived);
    if (arrived == 6) {
      CLOB();
      if (l < 16) {
        v2f sacc = *lds<v2f>(L_QR + l * 8);
#pragma unroll
        for (int u = 1; u < 5; ++u) sacc += *lds<v2f>(L_QR + u * 128 + l * 8);
        stc((v4u){f2u(sacc[0]), tag, f2u(sacc[1]), tag}, rws, l * 16, WS_Q1 + h * 2048 + (j & 1) * 1024 + (j >> 1) * 256);
      }
    }
    STAMP(2);
  } else {
    BAR();
    __builtin_amdgcn_s_sleep(KV_DELAY);
    const char* src = (const char*)ckv + (size_t)chunk * 131072 + l * 16;
    u32 row = type * 64;
    for (u32 n = 0; n < 128; ++n) {
      u32 keep;
      asm volatile("s_waitcnt vmcnt(" STR(KV_WIN) ")\n\ts_mov_b32 %0, m0\n\ts_mov_b32 m0, %2\n\ts_nop 0\n\tglobal_load_lds_dwordx4 %1, off\n\ts_mov_b32 m0, %0"
                   : "=&s"(keep) : "v"(src + row * 1024), "s"(row * RS) : "memory");
      row = (row + 1) & 127;
    }
    STAMP(3);
  }

  {
    v4u p[4];
    for (u32 it = 0; it < SPIN_CAP; ++it) {
      CLOB();
      u32 bad = 0;
#pragma unroll
      for (int k = 0; k < 4; ++k) p[k] = ldc(rws, l * 16 + k * 1024, WS_QA + type * 32768 + w * 4096);
#pragma unroll
      for (int k = 0; k < 4; ++k) bad |= (p[k][1] ^ tag) | (p[k][3] ^ tag);
      if (__all(bad == 0)) break;
      __builtin_amdgcn_s_sleep(4);
    }
#pragma unroll
    for (int k = 0; k < 4; ++k) *lds<v2u>(L_QA + (2 * w + (k >> 1)) * RS + (k & 1) * 512 + l * 8) = (v2u){p[k][0], p[k][2]};
  }
  STAMP(5);
  VM0();
  const bool ffn = (r < 27) && (w < 5);
  const u32 f0 = 216 * xc + 8 * r;
  v4u uv[2], a1[2];
  float x1[2];
  {
    const rsrc_t ru = mk(wuv, 512 * 4096 * 2);
#pragma unroll
    for (int t = 0; t < 2; ++t) {
      const u32 k = 8 * w + 4 * t + g;
      const u32 G = w & 3, a = w >> 2, sg = ((G & 1) << 1) | (G >> 1);
      const u32 c = 8 * ((j >> 2) * 32 + (j & 3) * 2 + a + 8 * sg) + ((k & 7) ^ ((G & 1) << 2));
      uv[t] = ldw(ru, c * 8192 + l15 * 16, h * 256);
    }
    const rsrc_t r1 = mk(w1, ffn ? 640 * 1728 * 2 : 0);
#pragma unroll
    for (int t = 0; t < 2; ++t) { a1[t] = ldw(r1, (128 * w + 64 * t + l) * 3456, f0 * 2); x1[t] = ldx(rx, 128 * w + 64 * t + l); }
  }
  BAR();
  STAMP(6);

  v4f sacc = {0.f, 0.f, 0.f, 0.f};
  {
    const u32 ab = L_KV + (16 * w + l15) * RS + g * 256, bb = L_QA + l15 * RS + g * 256;
#pragma unroll
    for (int s = 0; s < 16; ++s) sacc = mfma(*lds<v4u>(ab + s * 16), *lds<v4u>(bb + s * 16), sacc);
  }
  v2u tv[2][2][4][2];
  {
    const u32 q = l15 >> 2, p = l & 3, sg = ((p & 1) << 1) | (p >> 1);
    const u32 base = L_KV + (4 * g + q) * RS + ((w >> 2) * 32 + (w & 3) * 2 + 8 * sg) * 16;
#pragma unroll
    for (int a = 0; a < 2; ++a)
#pragma unroll
      for (int T = 0; T < 2; ++T)
#pragma unroll
        for (int s = 0; s < 4; ++s)
#pragma unroll
          for (int t = 0; t < 2; ++t) {
            v4s rv = __builtin_amdgcn_ds_read_tr16_b64_v4i16(lds<v4s>(base + 8 * ((p & 1) ^ T) + (32 * s + 16 * t) * RS + a * 16));
            tv[a][T][s][t] = *(v2u*)&rv;
          }
  }
  float mx = fmaxf(fmaxf(sacc[0], sacc[1]), fmaxf(sacc[2], sacc[3]));
  mx = mx32(mx16(mx));
  if (g == 0) *lds<float>(L_MX + l15 * 32 + w * 4) = mx;
  BAR();
  {
    v4f m0 = *lds<v4f>(L_MX + l15 * 32), m1 = *lds<v4f>(L_MX + l15 * 32 + 16);
    mx = fmaxf(fmaxf(fmaxf(m0[0], m0[1]), fmaxf(m0[2], m0[3])), fmaxf(fmaxf(m1[0], m1[1]), fmaxf(m1[2], m1[3])));
  }
  {
    const float e0 = __builtin_amdgcn_exp2f(sacc[0] - mx), e1 = __builtin_amdgcn_exp2f(sacc[1] - mx);
    const float e2 = __builtin_amdgcn_exp2f(sacc[2] - mx), e3 = __builtin_amdgcn_exp2f(sacc[3] - mx);
    *lds<v2u>(L_P + (w >> 1) * 1024 + l * 16 + (w & 1) * 8) = (v2u){pk2(e0, e1), pk2(e2, e3)};
    float ls = (e0 + e1) + (e2 + e3);
    ls = tr16(ls, ls); ls = tr32(ls, ls);
    if (g == 0) *lds<float>(L_LS + l15 * 32 + w * 4) = ls;
  }
  BAR();
  STAMP(7);
  {
    v4u pf[4];
#pragma unroll
    for (int s = 0; s < 4; ++s) pf[s] = *lds<v4u>(L_P + s * 1024 + l * 16);
    if (w == 0) {
      v4f a = *lds<v4f>(L_LS + l15 * 32), b = *lds<v4f>(L_LS + l15 * 32 + 16);
      const float lt = ((a[0] + a[1]) + (a[2] + a[3])) + ((b[0] + b[1]) + (b[2] + b[3]));
      if (g == 0) stc2((v2u){canon(f2u(mx)), canon(f2u(lt))}, rws, l15 * 1024, WS_ML + type * 16384 + chunk * 8);
    }
#pragma unroll
    for (int a = 0; a < 2; ++a) {
      v4f o[2];
#pragma unroll
      for (int T = 0; T < 2; ++T) {
        o[T] = (v4f){0.f, 0.f, 0.f, 0.f};
#pragma unroll
        for (int s = 0; s < 4; ++s) {
          v4u av = {tv[a][T][s][0][0], tv[a][T][s][0][1], tv[a][T][s][1][0], tv[a][T][s][1][1]};
          o[T] = mfma(av, pf[s], o[T]);
        }
      }
      v4u ov = {canon(pk2(o[0][0], o[0][1])), canon(pk2(o[0][2], o[0][3])), canon(pk2(o[1][0], o[1][1])), canon(pk2(o[1][2], o[1][3]))};
      stc(ov, rws, l15 * 131072 + g * 16 + a * 64, WS_OP + (type * 128 + w) * 16384 + chunk * 128);
    }
  }
  STAMP(8);
#pragma unroll
  for (int t = 0; t < 2; ++t) *lds<v4u>(L_QA + (8 * w + 4 * t + g) * UVS + l15 * 16) = uv[t];
  v4u ow[4], a2[2];
  {
    const rsrc_t ro = mk(wo, w < 5 ? 4096 * 640 * 2 : 0);
#pragma unroll
    for (int t = 0; t < 4; ++t) ow[t] = ldw(ro, (4 * g + t) * 1280 + l15 * 16, (h * 128 + j * 16) * 1280 + w * 256);
    const rsrc_t r2 = mk(w2, ffn ? 1728 * 640 * 2 : 0);
#pragma unroll
    for (int t = 0; t < 2; ++t) a2[t] = ldw(r2, (2 * g + t) * 1280 + l15 * 16, f0 * 1280 + w * 256);
  }
  if (w < 5) {
    float a[8] = {0.f, 0.f, 0.f, 0.f, 0.f, 0.f, 0.f, 0.f};
#pragma unroll
    for (int t = 0; t < 2; ++t) fma8(a, a1[t], rbf(rbf(x1[t] - mean_b) / sd_b));
    float o0, o1;
    red8(a, o0, o1);
    o0 = sum16(o0); o1 = sum16(o1);
    if (l15 == 0) *lds<v2f>(L_HR + w * 32 + g * 8) = (v2f){o0, o1};
  }
  STAMP(9);
  {
    v4u op[2], mlall; v2u mlo[2];
    for (u32 it = 0; it < SPIN_CAP; ++it) {
      CLOB();
      mlall = ldc(rws, l * 16, WS_ML + h * 1024);
#pragma unroll
      for (int t = 0; t < 2; ++t) mlo[t] = ldc2(rws, (l >> 3) * 8 + t * 64, WS_ML + h * 1024 + w * 128);
#pragma unroll
      for (int t = 0; t < 2; ++t) op[t] = ldc(rws, l * 16 + t * 1024, WS_OP + (h * 8 + j) * 16384 + w * 2048);
      const u32 mm = max3u(max3u(max4u(mlall), max4u(op[0]), max4u(op[1])), mlo[0][0], mlo[1][0]);
      if (__all(mm != SENT)) break;
    }
    STAMP(10);
    const float M = max64(fmaxf(u2f(mlall[0]), u2f(mlall[2])));
    const float L = sum64(__builtin_amdgcn_exp2f(u2f(mlall[0]) - M) * u2f(mlall[1]) + __builtin_amdgcn_exp2f(u2f(mlall[2]) - M) * u2f(mlall[3]));
    float a[8] = {0.f, 0.f, 0.f, 0.f, 0.f, 0.f, 0.f, 0.f};
#pragma unroll
    for (int t = 0; t < 2; ++t) fma8(a, op[t], __builtin_amdgcn_exp2f(u2f(mlo[t][0]) - M));
    float o0, o1;
    red8(a, o0, o1);
    o0 += dpp<DPP_ROR8>(o0); o1 += dpp<DPP_ROR8>(o1);
    const float inv = 1.f / L;
    o0 *= inv; o1 *= inv;
    const u32 hi = pk2(o0, o1), lo = pk2(o0 - lo2f(hi), o1 - hi2f(hi));
    *lds<u32>(L_OA + (2 * w + ((l >> 3) & 1)) * 144 + ((l & 7) * 8 + g * 2) * 2) = (l & 8) ? lo : hi;
  }
  BAR();
  {
    v4f c = {0.f, 0.f, 0.f, 0.f};
#pragma unroll
    for (int s = 0; s < 2; ++s) {
      const u32 ta = L_QA + (32 * s + 8 * g + (l15 >> 2)) * UVS + w * 32 + (l & 3) * 8;
      v4s r0 = __builtin_amdgcn_ds_read_tr16_b64_v4i16(lds<v4s>(ta));
      v4s r1 = __builtin_amdgcn_ds_read_tr16_b64_v4i16(lds<v4s>(ta + 4 * UVS));
      v2u b0 = *(v2u*)&r0, b1 = *(v2u*)&r1;
      c = mfma(*lds<v4u>(L_OA + l15 * 144 + s * 64 + g * 16), (v4u){b0[0], b0[1], b1[0], b1[1]}, c);
    }
    float cs = (c[0] + c[1]) + (c[2] + c[3]);
    cs = tr16(cs, cs); cs = tr32(cs, cs);
    const u32 cu = canon(f2u(cs));
    const v4u cv = {(u32)__builtin_amdgcn_mov_dpp(cu, 0x00, 0xF, 0xF, true), (u32)__builtin_amdgcn_mov_dpp(cu, 0x55, 0xF, 0xF, true),
                    (u32)__builtin_amdgcn_mov_dpp(cu, 0xAA, 0xF, 0xF, true), (u32)__builtin_amdgcn_mov_dpp(cu, 0xFF, 0xF, 0xF, true)};
    if (l < 16 && (l & 3) == 0) stc(cv, rws, l * 4, WS_CX + h * 4096 + w * 512 + j * 64);
  }
  STAMP(11);
  if (w < 5) {
    float y0, y1;
    {
      float hv[2];
      v2f hs = *lds<v2f>(L_HR + g * 8);
#pragma unroll
      for (int u = 1; u < 5; ++u) hs += *lds<v2f>(L_HR + u * 32 + g * 8);
      hv[0] = fmaxf(rbf(hs[0]), 0.f); hv[1] = fmaxf(rbf(hs[1]), 0.f);
      float a[8] = {0.f, 0.f, 0.f, 0.f, 0.f, 0.f, 0.f, 0.f};
#pragma unroll
      for (int t = 0; t < 2; ++t) fma8(a, a2[t], hv[t]);
      red8(a, y0, y1);
    }
    v4u cx;
    for (u32 it = 0; it < SPIN_CAP; ++it) {
      CLOB();
      cx = ldc(rws, (l & 7) * 64 + g * 16, WS_CX + h * 4096 + j * 512);
      if (__all(max4u(cx) != SENT)) break;
    }
    STAMP(12);
    float a[8] = {0.f, 0.f, 0.f, 0.f, 0.f, 0.f, 0.f, 0.f};
#pragma unroll
    for (int t = 0; t < 4; ++t) {
      float c = u2f(cx[t]);
      c += dpp<DPP_X1>(c); c += dpp<DPP_X2>(c); c += dpp<DPP_HMIR>(c);
      fma8(a, ow[t], rbf(c));
    }
    float s0, s1;
    red8(a, s0, s1);
    stc((v4u){canon(f2u(s0)), canon(f2u(y0)), canon(f2u(s1)), canon(f2u(y1))}, rws, l15 * 16384 + g * 16, WS_F + w * 16 * 16384 + bi * 64);
    STAMP(13);
  } else if (w < 7 && bi < 160) {
    const u32 rid = 2 * bi + (w - 5);
    const u32 xv = __builtin_amdgcn_raw_buffer_load_b32(rx, rid * 4, 0, 0);
    v4u f[4];
    for (u32 it = 0; it < SPIN_CAP; ++it) {
      CLOB();
#pragma unroll
      for (int t = 0; t < 4; ++t) f[t] = ldc(rws, l * 64 + t * 4096, WS_F + (rid >> 2) * 16384 + (rid & 3) * 16);
      if (__all(max3u(max3u(max4u(f[0]), max4u(f[1]), max4u(f[2])), max4u(f[3]), 0u) != SENT)) break;
    }
    STAMP(12);
    float v[4];
#pragma unroll
    for (int e = 0; e < 4; ++e) v[e] = (u2f(f[0][e]) + u2f(f[1][e])) + (u2f(f[2][e]) + u2f(f[3][e]));
    float sv = tr32(v[0], v[2]), yv = tr32(v[1], v[3]);
    sv = sum16(sv); yv = sum16(yv);
    sv = rbf(tr16(sv, sv)); yv = rbf(tr16(yv, yv));
    const float gt = rbf(yv / (1.f + __builtin_amdgcn_exp2f(-1.4426950408889634f * yv)));
    const float xx = (l < 32) ? lo2f(xv) : hi2f(xv);
    const u32 ob = pk2(xx + rbf(sv * gt), 0.f);
    if ((l & 31) == 0) out[2 * rid + (l >> 5)] = (u16)ob;
    if (rid == 0 && l == 0) __builtin_amdgcn_raw_buffer_store_b32(tag, rws, 0, WS_EPOCH, 16);
    STAMP(13);
  }
}

extern "C" void kernel_launch(void* const* d_in, const int* in_sizes, int n_in, void* d_out, int out_size, void* d_ws, size_t ws_size,
                              hipStream_t stream) {
  (void)in_sizes; (void)n_in; (void)out_size; (void)ws_size;
  hipFuncSetAttribute((const void*)mla_ffn_kernel, hipFuncAttributeMaxDynamicSharedMemorySize, L_TOTAL);
  mla_ffn_kernel<<<256, 512, L_TOTAL, stream>>>((const u16*)d_in[0], (const u16*)d_in[1], (const u16*)d_in[2], (const u16*)d_in[3],
                                                (const u16*)d_in[4], (const u16*)d_in[5], (const u16*)d_in[7], (const u16*)d_in[8],
                                                (u16*)d_out, (char*)d_ws);
}
```

```cpp
#define STAMPS
#include <cstdio>
#include <hip/hip_runtime.h>
#include <hip/hip_bf16.h>
#include <stdint.h>

typedef unsigned short u16;
typedef unsigned int u32;
typedef short v4s __attribute__((ext_vector_type(4)));
typedef __bf16 v8bf __attribute__((ext_vector_type(8)));
typedef __bf16 v2bf __attribute__((ext_vector_type(2)));
typedef float v4f __attribute__((ext_vector_type(4)));
typedef float v2f __attribute__((ext_vector_type(2)));
typedef u32 v4u __attribute__((ext_vector_type(4)));
typedef u32 v2u __attribute__((ext_vector_type(2)));
#define LAS __attribute__((address_space(3)))
typedef __amdgpu_buffer_rsrc_t rsrc_t;

#define KV_WIN 24
#define KV_DELAY 0
#define W_AUX 0
#define WARM 0
#define SL1 2
#define DL2 8
#define SL2 8
#define SL3 4
#define SL4 2
#define SL5 2
#define DL5 6
#define STR2(x) #x
#define STR(x) STR2(x)

constexpr u32 WS_EPOCH = 0;
constexpr u32 WS_Q1 = 4096;
constexpr u32 WS_QA = WS_Q1 + 65536;
constexpr u32 WS_ML = WS_QA + 65536;
constexpr u32 WS_CX = WS_ML + 32768;
constexpr u32 WS_F = WS_CX + 131072;
constexpr u32 WS_OP = WS_F + 80 * 16384;
constexpr u32 WS_END = WS_OP + 256 * 16384;
constexpr u32 WS_STAMP = WS_END;

constexpr u32 RS = 1040;
constexpr u32 L_KV = 0;
constexpr u32 L_QA = 128 * RS;
constexpr u32 L_P = L_QA + 17408;
constexpr u32 L_MX = L_P + 4096;
constexpr u32 L_LS = L_MX + 512;
constexpr u32 L_QR = L_LS + 512;
constexpr u32 L_HR = L_QR + 640;
constexpr u32 L_OA = L_HR + 192;
constexpr u32 L_CNT = L_OA + 16 * 144;
constexpr u32 L_TOTAL = L_CNT + 64;
constexpr u32 UVS = 272;
static_assert(64 * UVS <= 17408, "uv image");
static_assert(L_TOTAL <= 163840, "lds");

constexpr u32 SENT = 0xFFFFFFFFu;
constexpr u32 SPIN_CAP = 1u << 18;

__device__ __forceinline__ float u2f(u32 u) { return __uint_as_float(u); }
__device__ __forceinline__ u32 f2u(float f) { return __float_as_uint(f); }
__device__ __forceinline__ float lo2f(u32 u) { return __uint_as_float(u << 16); }
__device__ __forceinline__ float hi2f(u32 u) { return __uint_as_float(u & 0xFFFF0000u); }
__device__ __forceinline__ u32 pk2(float a, float b) { v2f f = {a, b}; v2bf c = __builtin_convertvector(f, v2bf); return *(u32*)&c; }
__device__ __forceinline__ float rbf(float a) { return lo2f(pk2(a, a)); }
__device__ __forceinline__ u32 canon(u32 u) { return u < 0xFFFFFFFEu ? u : 0xFFFFFFFEu; }
__device__ __forceinline__ u32 max3u(u32 a, u32 b, u32 c) { u32 m = a > b ? a : b; return m > c ? m : c; }
__device__ __forceinline__ u32 max4u(v4u v) { return max3u(max3u(v[0], v[1], v[2]), v[3], v[3]); }
template <int CTRL> __device__ __forceinline__ float dpp(float v) { return u2f(__builtin_amdgcn_mov_dpp(f2u(v), CTRL, 0xF, 0xF, true)); }
constexpr int DPP_X1 = 0xB1, DPP_X2 = 0x4E, DPP_ROR4 = 0x124, DPP_ROR8 = 0x128, DPP_HMIR = 0x141;
__device__ __forceinline__ float sum16(float v) { v += dpp<DPP_X1>(v); v += dpp<DPP_X2>(v); v += dpp<DPP_ROR4>(v); v += dpp<DPP_ROR8>(v); return v; }
__device__ __forceinline__ float max16(float v) { v = fmaxf(v, dpp<DPP_X1>(v)); v = fmaxf(v, dpp<DPP_X2>(v)); v = fmaxf(v, dpp<DPP_ROR4>(v)); v = fmaxf(v, dpp<DPP_ROR8>(v)); return v; }
__device__ __forceinline__ float tr16(float a, float b) { auto r = __builtin_amdgcn_permlane16_swap(f2u(a), f2u(b), false, false); u32 x = r[0], y = r[1]; return u2f(x) + u2f(y); }
__device__ __forceinline__ float tr32(float a, float b) { auto r = __builtin_amdgcn_permlane32_swap(f2u(a), f2u(b), false, false); u32 x = r[0], y = r[1]; return u2f(x) + u2f(y); }
__device__ __forceinline__ float mx16(float a) { auto r = __builtin_amdgcn_permlane16_swap(f2u(a), f2u(a), false, false); u32 x = r[0], y = r[1]; return fmaxf(u2f(x), u2f(y)); }
__device__ __forceinline__ float mx32(float a) { auto r = __builtin_amdgcn_permlane32_swap(f2u(a), f2u(a), false, false); u32 x = r[0], y = r[1]; return fmaxf(u2f(x), u2f(y)); }
__device__ __forceinline__ float sum64(float v) { v = sum16(v); v = tr16(v, v); return tr32(v, v); }
__device__ __forceinline__ float max64(float v) { v = max16(v); v = mx16(v); return mx32(v); }
__device__ __forceinline__ float dot2(u32 a, u32 b, float c) { return __builtin_amdgcn_fdot2_f32_bf16(*(v2bf*)&a, *(v2bf*)&b, c, false); }
__device__ __forceinline__ v4f mfma(v4u a, v4u b, v4f c) { return __builtin_amdgcn_mfma_f32_16x16x32_bf16(*(v8bf*)&a, *(v8bf*)&b, c, 0, 0, 0); }
__device__ __forceinline__ rsrc_t mk(const void* p, u32 n) { return __builtin_amdgcn_make_buffer_rsrc((void*)p, 0, n, 0x00020000); }
__device__ __forceinline__ v4u ldw(rsrc_t r, u32 v, u32 s) { return __builtin_amdgcn_raw_buffer_load_b128(r, v, s, W_AUX); }
__device__ __forceinline__ v4u ldc(rsrc_t r, u32 v, u32 s) { return __builtin_amdgcn_raw_buffer_load_b128(r, v, s, 16); }
__device__ __forceinline__ v2u ldc2(rsrc_t r, u32 v, u32 s) { return __builtin_amdgcn_raw_buffer_load_b64(r, v, s, 16); }
__device__ __forceinline__ void stc(v4u d, rsrc_t r, u32 v, u32 s) { __builtin_amdgcn_raw_buffer_store_b128(d, r, v, s, 16); }
__device__ __forceinline__ void stc2(v2u d, rsrc_t r, u32 v, u32 s) { __builtin_amdgcn_raw_buffer_store_b64(d, r, v, s, 16); }
__device__ __forceinline__ u16 ldx(rsrc_t r, u32 elem) { return __builtin_amdgcn_raw_buffer_load_b16(r, elem * 2, 0, 0); }
#define BAR() asm volatile("s_waitcnt lgkmcnt(0)\n\ts_barrier" ::: "memory")
#define SCHED() __builtin_amdgcn_sched_barrier(0)
#define CLOB() asm volatile("" ::: "memory")
#define VM0() asm volatile("s_waitcnt vmcnt(0)" ::: "memory")
template <typename T> __device__ __forceinline__ LAS T* lds(u32 off) { return (LAS T*)(uintptr_t)off; }
__device__ __forceinline__ void fma8(float* a, v4u w, float x) {
#pragma unroll
  for (int e = 0; e < 4; ++e) { a[2 * e] = fmaf(lo2f(w[e]), x, a[2 * e]); a[2 * e + 1] = fmaf(hi2f(w[e]), x, a[2 * e + 1]); }
}
__device__ __forceinline__ void red8(const float* a, float& o0, float& o1) {
  float t0 = tr32(a[0], a[4]), t1 = tr32(a[1], a[5]), t2 = tr32(a[2], a[6]), t3 = tr32(a[3], a[7]);
  o0 = tr16(t0, t2); o1 = tr16(t1, t3);
}

#define NST 24
#define STAMP(k) do { st[k] = (u32)__builtin_amdgcn_s_memrealtime(); } while (0)

__global__ void __launch_bounds__(512, 2)
mla_ffn_kernel(const u16* x, const u16* ckv, const u16* wq, const u16* wuk,
               const u16* wuv, const u16* wo, const u16* w1, const u16* w2,
               u16* out, char* ws) {
  extern __shared__ __attribute__((aligned(16))) char smem_[];
  u32 st[NST];
#pragma unroll
  for (int k = 0; k < NST; ++k) st[k] = 0;
  STAMP(0);
  const unsigned long long clk0 = __builtin_amdgcn_s_memtime();
  asm volatile("" :: "s"(x), "s"(ckv), "s"(wq), "s"(wuk), "s"(wuv), "s"(wo), "s"(w1), "s"(w2), "s"(out), "s"(ws));
  STAMP(14);
  const u32 bi = blockIdx.x, xc = bi & 7, r = bi >> 3;
  const u32 type = r & 1;
  const u32 chunk = xc * 16 + (r >> 1);
  const u32 h = (1 - type) * 16 + xc * 2 + (r >> 4), j = (r >> 1) & 7;
  const u32 w = __builtin_amdgcn_readfirstlane(threadIdx.x >> 6), l = threadIdx.x & 63;
  const u32 l15 = l & 15, g = l >> 4;
  const rsrc_t rws = mk(ws, WS_END);
  const rsrc_t rx = mk(x, 1280);
  u32 tag;
  float mean_b = 0.f, sd_b = 1.f;
  if (threadIdx.x == 0) { *lds<v2u>(L_CNT) = (v2u){0u, 0u}; }

  if (w < 5) {
    tag = __builtin_amdgcn_raw_buffer_load_b32(rws, 0, WS_EPOCH, 16);
    u32 xs[5];
#pragma unroll
    for (int k = 0; k < 5; ++k) xs[k] = __builtin_amdgcn_raw_buffer_load_b32(rx, l * 4 + k * 256, 0, 0);
    const u32 m0 = 320 * (j & 1) + 64 * w + (l >> 2);
    u16 xr[4];
#pragma unroll
    for (int t = 0; t < 4; ++t) xr[t] = ldx(rx, m0 + 16 * t);
    const rsrc_t rq = mk(wq, 640 * 4096 * 2);
    v4u qv[4];
#pragma unroll
    for (int t = 0; t < 4; ++t) qv[t] = ldw(rq, m0 * 8192 + (l & 3) * 16 + t * 16 * 8192, h * 256 + (j >> 1) * 64);
    const rsrc_t rk = mk(wuk, w < 4 ? 512 * 4096 * 2 : 0);
    v4u kv[4];
#pragma unroll
    for (int t = 0; t < 4; ++t) kv[t] = ldw(rk, (4 * g + t) * 8192 + l15 * 16, (64 * j + 16 * w) * 8192 + h * 256);
    SCHED();
    BAR();
    SCHED();
    float s = 0.f;
#pragma unroll
    for (int k = 0; k < 5; ++k) s += lo2f(xs[k]) + hi2f(xs[k]);
    const float mean = sum64(s) * (1.f / 640.f);
    float v = 0.f;
#pragma unroll
    for (int k = 0; k < 5; ++k) { float a = lo2f(xs[k]) - mean, b = hi2f(xs[k]) - mean; v = fmaf(a, a, v); v = fmaf(b, b, v); }
    v = rbf(sum64(v) * (1.f / 640.f));
    mean_b = rbf(mean);
    sd_b = rbf(sqrtf(rbf(v + 1e-5f)));
    STAMP(1);
    float a[8] = {0.f, 0.f, 0.f, 0.f, 0.f, 0.f, 0.f, 0.f};
#pragma unroll
    for (int t = 0; t < 4; ++t) fma8(a, qv[t], rbf(rbf(lo2f(xr[t]) - mean_b) / sd_b));
    float o0, o1;
    red8(a, o0, o1);
    o0 += dpp<DPP_ROR4>(o0); o1 += dpp<DPP_ROR4>(o1);
    o0 += dpp<DPP_ROR8>(o0); o1 += dpp<DPP_ROR8>(o1);
    if (l15 < 4) *lds<v2f>(L_QR + w * 128 + (l15 * 8 + g * 2) * 4) = (v2f){o0, o1};
    asm volatile("s_waitcnt lgkmcnt(0)" ::: "memory");
    asm volatile("" : "+v"(tag));
    tag += 1u;
    u32 arrived = 0;
    if (l == 0) arrived = __hip_atomic_fetch_add(lds<u32>(L_CNT), 1u, __ATOMIC_RELAXED, __HIP_MEMORY_SCOPE_WORKGROUP);
    arrived = __builtin_amdgcn_readfirstlane(arrived);
    if (arrived == 6) {
      CLOB();
      if (l < 16) {
        v2f sacc = *lds<v2f>(L_QR + l * 8);
#pragma unroll
        for (int u = 1; u < 5; ++u) sacc += *lds<v2f>(L_QR + u * 128 + l * 8);
        stc((v4u){f2u(sacc[0]), tag, f2u(sacc[1]), tag}, rws, l * 16, WS_Q1 + h * 2048 + (j & 1) * 1024 + (j >> 1) * 256);
      }
    }
    STAMP(2);
    if (w < 4) {
      v4u p0, p1;
      _Pragma("unroll 1") for (u32 it = 0; it < SPIN_CAP; ++it) {
        CLOB();
        p0 = ldc(rws, (l15 * 4 + g) * 16, WS_Q1 + h * 2048);
        p1 = ldc(rws, (l15 * 4 + g) * 16 + 1024, WS_Q1 + h * 2048);
        const bool ok = ((p0[1] ^ tag) | (p0[3] ^ tag) | (p1[1] ^ tag) | (p1[3] ^ tag)) == 0;
        if (__all(ok)) break;
        __builtin_amdgcn_s_sleep(SL1);
      }
      STAMP(3);
      const u32 qp = pk2(u2f(p0[0]) + u2f(p1[0]), u2f(p0[2]) + u2f(p1[2]));
      auto s16 = __builtin_amdgcn_permlane16_swap(qp, qp, false, false);
      const u32 e02 = s16[0], e13 = s16[1];
      auto sa = __builtin_amdgcn_permlane32_swap(e02, e02, false, false);
      auto sb = __builtin_amdgcn_permlane32_swap(e13, e13, false, false);
      const u32 q0 = sa[0], q2 = sa[1], q1 = sb[0], q3 = sb[1];
      float d[4];
#pragma unroll
      for (int t = 0; t < 4; ++t) {
        float acc = dot2(kv[t][0], q0, 0.f);
        acc = dot2(kv[t][1], q1, acc); acc = dot2(kv[t][2], q2, acc); acc = dot2(kv[t][3], q3, acc);
        d[t] = sum16(acc) * (0.08838834764831845f * 1.4426950408889634f);
      }
      if (l15 == 0) stc((v4u){pk2(d[0], d[1]), tag, pk2(d[2], d[3]), tag}, rws, g * 16, WS_QA + h * 2048 + j * 256 + w * 64);
      if (l == 0) __hip_atomic_fetch_add(lds<u32>(L_CNT + 4), 1u, __ATOMIC_RELAXED, __HIP_MEMORY_SCOPE_WORKGROUP);
      STAMP(4);
    }
  } else if (w < 7) {
    const v4u sv = {SENT, SENT, SENT, SENT};
    tag = __builtin_amdgcn_raw_buffer_load_b32(rws, 0, WS_EPOCH, 16);
    const u32 t0 = (w - 5) * 64 + l;
    BAR();
#pragma unroll
    for (int k = 0; k < 8; ++k) stc(sv, rws, t0 * 16 + k * 2048, WS_OP + (h * 8 + j) * 16384);
#pragma unroll
    for (int k = 0; k < 2; ++k) stc(sv, rws, t0 * 16 + k * 2048, WS_F + bi * 5120);
    if (w == 5) stc(sv, rws, l * 16 + 4096, WS_F + bi * 5120);
    else if (l < 32) stc(sv, rws, (l >> 2) * 512 + (l & 3) * 16, WS_CX + h * 4096 + j * 64);
    else if (l < 40) stc(sv, rws, (l - 32) * 16, WS_ML + h * 1024 + j * 128);
    VM0();
    asm volatile("" : "+v"(tag));
    tag += 1u;
    u32 arrived = 0;
    if (l == 0) arrived = __hip_atomic_fetch_add(lds<u32>(L_CNT), 1u, __ATOMIC_RELAXED, __HIP_MEMORY_SCOPE_WORKGROUP);
    arrived = __builtin_amdgcn_readfirstlane(arrived);
    if (arrived == 6) {
      CLOB();
      if (l < 16) {
        v2f sacc = *lds<v2f>(L_QR + l * 8);
#pragma unroll
        for (int u = 1; u < 5; ++u) sacc += *lds<v2f>(L_QR + u * 128 + l * 8);
        stc((v4u){f2u(sacc[0]), tag, f2u(sacc[1]), tag}, rws, l * 16, WS_Q1 + h * 2048 + (j & 1) * 1024 + (j >> 1) * 256);
      }
    }
    STAMP(2);
  } else {
    tag = __builtin_amdgcn_raw_buffer_load_b32(rws, 0, WS_EPOCH, 16);
    BAR();
    asm volatile("" : "+v"(tag));
    tag += 1u;
    STAMP(1);
    __builtin_amdgcn_s_sleep(KV_DELAY);
    const char* src = (const char*)ckv + (size_t)chunk * 131072 + l * 16;
    u32 row = type * 64;
#pragma unroll 1
    for (u32 n = 0; n < 128; ++n) {
      u32 keep;
      asm volatile("s_waitcnt vmcnt(" STR(KV_WIN) ")\n\ts_mov_b32 %0, m0\n\ts_mov_b32 m0, %2\n\ts_nop 0\n\tglobal_load_lds_dwordx4 %1, off\n\ts_mov_b32 m0, %0"
                   : "=&s"(keep) : "v"(src + row * 1024), "s"(row * RS) : "memory");
      row = (row + 1) & 127;
    }
    STAMP(3);
  }

  _Pragma("unroll 1") for (u32 it = 0; it < SPIN_CAP * 16; ++it) {
    if (__hip_atomic_load(lds<u32>(L_CNT + 4), __ATOMIC_RELAXED, __HIP_MEMORY_SCOPE_WORKGROUP) != 0) break;
    __builtin_amdgcn_s_sleep(2);
  }
  __builtin_amdgcn_s_sleep(DL2);
  STAMP(15);
  {
    v4u p[4];
    _Pragma("unroll 1") for (u32 it = 0; it < SPIN_CAP; ++it) {
      CLOB();
      u32 bad = 0;
#pragma unroll
      for (int k = 0; k < 4; ++k) p[k] = ldc(rws, l * 16 + k * 1024, WS_QA + type * 32768 + w * 4096);
#pragma unroll
      for (int k = 0; k < 4; ++k) bad |= (p[k][1] ^ tag) | (p[k][3] ^ tag);
      if (__all(bad == 0)) break;
      __builtin_amdgcn_s_sleep(SL2);
    }
#pragma unroll
    for (int k = 0; k < 4; ++k) *lds<v2u>(L_QA + (2 * w + (k >> 1)) * RS + (k & 1) * 512 + l * 8) = (v2u){p[k][0], p[k][2]};
  }
  STAMP(5);
  VM0();
  const bool ffn = (r < 27) && (w < 5);
  const u32 f0 = 216 * xc + 8 * r;
  v4u uv[2], a1[2];
  u16 x1[2];
  {
    const rsrc_t ru = mk(wuv, 512 * 4096 * 2);
#pragma unroll
    for (int t = 0; t < 2; ++t) {
      const u32 k = 8 * w + 4 * t + g;
      const u32 G = w & 3, a = w >> 2, sg = ((G & 1) << 1) | (G >> 1);
      const u32 c = 8 * ((j >> 2) * 32 + (j & 3) * 2 + a + 8 * sg) + ((k & 7) ^ ((G & 1) << 2));
      uv[t] = ldw(ru, c * 8192 + l15 * 16, h * 256);
    }
    const rsrc_t r1 = mk(w1, ffn ? 640 * 1728 * 2 : 0);
#pragma unroll
    for (int t = 0; t < 2; ++t) { a1[t] = ldw(r1, (128 * w + 64 * t + l) * 3456, f0 * 2); x1[t] = ldx(rx, 128 * w + 64 * t + l); }
  }
  BAR();
  STAMP(6);

  v4f sacc = {0.f, 0.f, 0.f, 0.f};
  {
    const u32 ab = L_KV + (16 * w + l15) * RS + g * 256, bb = L_QA + l15 * RS + g * 256;
#pragma unroll
    for (int s = 0; s < 16; ++s) sacc = mfma(*lds<v4u>(ab + s * 16), *lds<v4u>(bb + s * 16), sacc);
  }
  asm volatile("" : "+v"(sacc));
  STAMP(16);
  v2u tv[2][2][4][2];
  {
    const u32 q = l15 >> 2, p = l & 3, sg = ((p & 1) << 1) | (p >> 1);
    const u32 base = L_KV + (4 * g + q) * RS + ((w >> 2) * 32 + (w & 3) * 2 + 8 * sg) * 16;
#pragma unroll
    for (int a = 0; a < 2; ++a)
#pragma unroll
      for (int T = 0; T < 2; ++T)
#pragma unroll
        for (int s = 0; s < 4; ++s)
#pragma unroll
          for (int t = 0; t < 2; ++t) {
            v4s rv = __builtin_amdgcn_ds_read_tr16_b64_v4i16(lds<v4s>(base + 8 * ((p & 1) ^ T) + (32 * s + 16 * t) * RS + a * 16));
            tv[a][T][s][t] = *(v2u*)&rv;
          }
  }
  float mx = fmaxf(fmaxf(sacc[0], sacc[1]), fmaxf(sacc[2], sacc[3]));
  mx = mx32(mx16(mx));
  if (g == 0) *lds<float>(L_MX + l15 * 32 + w * 4) = mx;
  BAR();
  STAMP(17);
  {
    v4f m0 = *lds<v4f>(L_MX + l15 * 32), m1 = *lds<v4f>(L_MX + l15 * 32 + 16);
    mx = fmaxf(fmaxf(fmaxf(m0[0], m0[1]), fmaxf(m0[2], m0[3])), fmaxf(fmaxf(m1[0], m1[1]), fmaxf(m1[2], m1[3])));
  }
  {
    const float e0 = __builtin_amdgcn_exp2f(sacc[0] - mx), e1 = __builtin_amdgcn_exp2f(sacc[1] - mx);
    const float e2 = __builtin_amdgcn_exp2f(sacc[2] - mx), e3 = __builtin_amdgcn_exp2f(sacc[3] - mx);
    *lds<v2u>(L_P + (w >> 1) * 1024 + l * 16 + (w & 1) * 8) = (v2u){pk2(e0, e1), pk2(e2, e3)};
    float ls = (e0 + e1) + (e2 + e3);
    ls = tr16(ls, ls); ls = tr32(ls, ls);
    if (g == 0) *lds<float>(L_LS + l15 * 32 + w * 4) = ls;
  }
  BAR();
  STAMP(7);
  {
    v4u pf[4];
#pragma unroll
    for (int s = 0; s < 4; ++s) pf[s] = *lds<v4u>(L_P + s * 1024 + l * 16);
    if (w == 0) {
      v4f a = *lds<v4f>(L_LS + l15 * 32), b = *lds<v4f>(L_LS + l15 * 32 + 16);
      const float lt = ((a[0] + a[1]) + (a[2] + a[3])) + ((b[0] + b[1]) + (b[2] + b[3]));
      if (g == 0) stc2((v2u){canon(f2u(mx)), canon(f2u(lt))}, rws, l15 * 1024, WS_ML + type * 16384 + chunk * 8);
    }
#pragma unroll
    for (int a = 0; a < 2; ++a) {
      v4f o[2];
#pragma unroll
      for (int T = 0; T < 2; ++T) {
        o[T] = (v4f){0.f, 0.f, 0.f, 0.f};
#pragma unroll
        for (int s = 0; s < 4; ++s) {
          v4u av = {tv[a][T][s][0][0], tv[a][T][s][0][1], tv[a][T][s][1][0], tv[a][T][s][1][1]};
          o[T] = mfma(av, pf[s], o[T]);
        }
      }
      v4u ov = {canon(pk2(o[0][0], o[0][1])), canon(pk2(o[0][2], o[0][3])), canon(pk2(o[1][0], o[1][1])), canon(pk2(o[1][2], o[1][3]))};
      stc(ov, rws, l15 * 131072 + g * 16 + a * 64, WS_OP + (type * 128 + w) * 16384 + chunk * 128);
    }
  }
  STAMP(8);
#pragma unroll
  for (int t = 0; t < 2; ++t) *lds<v4u>(L_QA + (8 * w + 4 * t + g) * UVS + l15 * 16) = uv[t];
  v4u ow[4], a2[2];
  {
    const rsrc_t ro = mk(wo, w < 5 ? 4096 * 640 * 2 : 0);
#pragma unroll
    for (int t = 0; t < 4; ++t) ow[t] = ldw(ro, (4 * g + t) * 1280 + l15 * 16, (h * 128 + j * 16) * 1280 + w * 256);
    const rsrc_t r2 = mk(w2, ffn ? 1728 * 640 * 2 : 0);
#pragma unroll
    for (int t = 0; t < 2; ++t) a2[t] = ldw(r2, (2 * g + t) * 1280 + l15 * 16, f0 * 1280 + w * 256);
  }
  if (w < 5) {
    float a[8] = {0.f, 0.f, 0.f, 0.f, 0.f, 0.f, 0.f, 0.f};
#pragma unroll
    for (int t = 0; t < 2; ++t) fma8(a, a1[t], rbf(rbf(lo2f(x1[t]) - mean_b) / sd_b));
    float o0, o1;
    red8(a, o0, o1);
    o0 = sum16(o0); o1 = sum16(o1);
    if (l15 == 0) *lds<v2f>(L_HR + w * 32 + g * 8) = (v2f){o0, o1};
  }
  STAMP(9);
  {
    v4u op[2], mlall; v2u mlo[2];
    _Pragma("unroll 1") for (u32 it = 0; it < SPIN_CAP; ++it) {
      CLOB();
      mlall = ldc(rws, l * 16, WS_ML + h * 1024);
#pragma unroll
      for (int t = 0; t < 2; ++t) mlo[t] = ldc2(rws, (l >> 3) * 8 + t * 64, WS_ML + h * 1024 + w * 128);
#pragma unroll
      for (int t = 0; t < 2; ++t) op[t] = ldc(rws, l * 16 + t * 1024, WS_OP + (h * 8 + j) * 16384 + w * 2048);
      const u32 mm = max3u(max3u(max4u(mlall), max4u(op[0]), max4u(op[1])), mlo[0][0], mlo[1][0]);
      if (__all(mm != SENT)) break;
      __builtin_amdgcn_s_sleep(SL3);
    }
    STAMP(10);
    const float M = max64(fmaxf(u2f(mlall[0]), u2f(mlall[2])));
    const float L = sum64(__builtin_amdgcn_exp2f(u2f(mlall[0]) - M) * u2f(mlall[1]) + __builtin_amdgcn_exp2f(u2f(mlall[2]) - M) * u2f(mlall[3]));
    float a[8] = {0.f, 0.f, 0.f, 0.f, 0.f, 0.f, 0.f, 0.f};
#pragma unroll
    for (int t = 0; t < 2; ++t) fma8(a, op[t], __builtin_amdgcn_exp2f(u2f(mlo[t][0]) - M));
    float o0, o1;
    red8(a, o0, o1);
    o0 += dpp<DPP_ROR8>(o0); o1 += dpp<DPP_ROR8>(o1);
    const float inv = 1.f / L;
    o0 *= inv; o1 *= inv;
    const u32 hi = pk2(o0, o1), lo = pk2(o0 - lo2f(hi), o1 - hi2f(hi));
    *lds<u32>(L_OA + (2 * w + ((l >> 3) & 1)) * 144 + ((l & 7) * 8 + g * 2) * 2) = (l & 8) ? lo : hi;
  }
  STAMP(18);
  BAR();
  STAMP(19);
  {
    v4f c = {0.f, 0.f, 0.f, 0.f};
#pragma unroll
    for (int s = 0; s < 2; ++s) {
      const u32 ta = L_QA + (32 * s + 8 * g + (l15 >> 2)) * UVS + w * 32 + (l & 3) * 8;
      v4s r0 = __builtin_amdgcn_ds_read_tr16_b64_v4i16(lds<v4s>(ta));
      v4s r1 = __builtin_amdgcn_ds_read_tr16_b64_v4i16(lds<v4s>(ta + 4 * UVS));
      v2u b0 = *(v2u*)&r0, b1 = *(v2u*)&r1;
      c = mfma(*lds<v4u>(L_OA + l15 * 144 + s * 64 + g * 16), (v4u){b0[0], b0[1], b1[0], b1[1]}, c);
    }
    float cs = (c[0] + c[1]) + (c[2] + c[3]);
    cs = tr16(cs, cs); cs = tr32(cs, cs);
    const u32 cu = canon(f2u(cs));
    const v4u cv = {(u32)__builtin_amdgcn_mov_dpp(cu, 0x00, 0xF, 0xF, true), (u32)__builtin_amdgcn_mov_dpp(cu, 0x55, 0xF, 0xF, true),
                    (u32)__builtin_amdgcn_mov_dpp(cu, 0xAA, 0xF, 0xF, true), (u32)__builtin_amdgcn_mov_dpp(cu, 0xFF, 0xF, 0xF, true)};
    if (l < 16 && (l & 3) == 0) stc(cv, rws, l * 4, WS_CX + h * 4096 + w * 512 + j * 64);
  }
  STAMP(11);
  if (w < 5) {
    float y0, y1;
    {
      float hv[2];
      v2f hs = *lds<v2f>(L_HR + g * 8);
#pragma unroll
      for (int u = 1; u < 5; ++u) hs += *lds<v2f>(L_HR + u * 32 + g * 8);
      hv[0] = fmaxf(rbf(hs[0]), 0.f); hv[1] = fmaxf(rbf(hs[1]), 0.f);
      float a[8] = {0.f, 0.f, 0.f, 0.f, 0.f, 0.f, 0.f, 0.f};
#pragma unroll
      for (int t = 0; t < 2; ++t) fma8(a, a2[t], hv[t]);
      red8(a, y0, y1);
    }
    v4u cx;
    _Pragma("unroll 1") for (u32 it = 0; it < SPIN_CAP; ++it) {
      CLOB();
      cx = ldc(rws, (l & 7) * 64 + g * 16, WS_CX + h * 4096 + j * 512);
      if (__all(max4u(cx) != SENT)) break;
      __builtin_amdgcn_s_sleep(SL4);
    }
    STAMP(12);
    float a[8] = {0.f, 0.f, 0.f, 0.f, 0.f, 0.f, 0.f, 0.f};
#pragma unroll
    for (int t = 0; t < 4; ++t) {
      float c = u2f(cx[t]);
      c += dpp<DPP_X1>(c); c += dpp<DPP_X2>(c); c += dpp<DPP_HMIR>(c);
      fma8(a, ow[t], rbf(c));
    }
    float s0, s1;
    red8(a, s0, s1);
    stc((v4u){canon(f2u(s0)), canon(f2u(y0)), canon(f2u(s1)), canon(f2u(y1))}, rws, l15 * 16384 + g * 16, WS_F + w * 16 * 16384 + bi * 64);
    if (l == 0) __hip_atomic_store(lds<u32>(L_CNT + 4), 100u, __ATOMIC_RELAXED, __HIP_MEMORY_SCOPE_WORKGROUP);
    STAMP(13);
  } else if (w < 7 && bi < 160) {
    const u32 rid = 2 * bi + (w - 5);
    const u32 xv = __builtin_amdgcn_raw_buffer_load_b32(rx, rid * 4, 0, 0);
    _Pragma("unroll 1") for (u32 it = 0; it < SPIN_CAP * 16; ++it) {
      if (__hip_atomic_load(lds<u32>(L_CNT + 4), __ATOMIC_RELAXED, __HIP_MEMORY_SCOPE_WORKGROUP) == 100u) break;
      __builtin_amdgcn_s_sleep(1);
    }
    __builtin_amdgcn_s_sleep(DL5);
    v4u f[4];
    _Pragma("unroll 1") for (u32 it = 0; it < SPIN_CAP; ++it) {
      CLOB();
#pragma unroll
      for (int t = 0; t < 4; ++t) f[t] = ldc(rws, l * 64 + t * 4096, WS_F + (rid >> 2) * 16384 + (rid & 3) * 16);
      if (__all(max3u(max3u(max4u(f[0]), max4u(f[1]), max4u(f[2])), max4u(f[3]), 0u) != SENT)) break;
      __builtin_amdgcn_s_sleep(SL5);
    }
    STAMP(12);
    float v[4];
#pragma unroll
    for (int e = 0; e < 4; ++e) v[e] = (u2f(f[0][e]) + u2f(f[1][e])) + (u2f(f[2][e]) + u2f(f[3][e]));
    float sv = tr32(v[0], v[2]), yv = tr32(v[1], v[3]);
    sv = sum16(sv); yv = sum16(yv);
    sv = rbf(tr16(sv, sv)); yv = rbf(tr16(yv, yv));
    const float gt = rbf(yv / (1.f + __builtin_amdgcn_exp2f(-1.4426950408889634f * yv)));
    const float xx = (l < 32) ? lo2f(xv) : hi2f(xv);
    const u32 ob = pk2(xx + rbf(sv * gt), 0.f);
    if ((l & 31) == 0) out[2 * rid + (l >> 5)] = (u16)ob;
    if (rid == 0 && l == 0) __builtin_amdgcn_raw_buffer_store_b32(tag, rws, 0, WS_EPOCH, 16);
    STAMP(13);
  }
  if (l == 0) {
    u32* sp = (u32*)(ws + WS_STAMP) + (bi * 8 + w) * NST;
    st[NST - 1] = (__builtin_amdgcn_s_getreg(63508) & 15) | (((u32)(__builtin_amdgcn_s_memtime() - clk0)) << 4);
    st[NST - 2] = (u32)__builtin_amdgcn_s_memrealtime();
#pragma unroll
    for (int k = 0; k < NST; ++k) sp[k] = st[k];
  }
}

__global__ void report_k(const char* ws) {
  const u32* st = (const u32*)(ws + WS_STAMP);
  __shared__ u32 mn[NST], mx[NST], sm[NST], ct[NST];
  const int t = threadIdx.x;
  if (t < NST) { mn[t] = 0xFFFFFFFFu; mx[t] = 0; sm[t] = 0; ct[t] = 0; }
  __syncthreads();
  for (int i = t; i < 2048; i += 256) atomicMin(&mn[0], st[i * NST]);
  __syncthreads();
  const u32 t0 = mn[0];
  __syncthreads();
  if (t == 0) mn[0] = 0;
  for (int i = t; i < 2048; i += 256)
    for (int k = 0; k < NST - 1; ++k) { u32 v = st[i * NST + k]; if (v) { v -= t0; atomicMin(&mn[k], v); atomicMax(&mx[k], v); atomicAdd(&sm[k], v); atomicAdd(&ct[k], 1u); } }
  __syncthreads();
  if (t == 0) {
    u32 now = (u32)__builtin_amdgcn_s_memrealtime() - t0;
    #define A(k) (ct[k] ? sm[k] / ct[k] : 0), mx[k]
    printf("S s0 %u/%u s1 %u/%u s2 %u/%u s3 %u/%u s5 %u/%u s6 %u/%u s8 %u/%u s10 %u/%u s13 %u/%u end %u now %u\n", A(0), A(1), A(2), A(3), A(5), A(6), A(8), A(10), A(13), mx[NST - 2], now);
  }
}

extern "C" void kernel_launch(void* const* d_in, const int* in_sizes, int n_in, void* d_out, int out_size, void* d_ws, size_t ws_size,
                              hipStream_t stream) {
  (void)in_sizes; (void)n_in; (void)out_size; (void)ws_size;
  (void)hipFuncSetAttribute((const void*)mla_ffn_kernel, hipFuncAttributeMaxDynamicSharedMemorySize, L_TOTAL);
  mla_ffn_kernel<<<256, 512, L_TOTAL, stream>>>((const u16*)d_in[0], (const u16*)d_in[1], (const u16*)d_in[2], (const u16*)d_in[3],
                                                (const u16*)d_in[4], (const u16*)d_in[5], (const u16*)d_in[7], (const u16*)d_in[8],
                                                (u16*)d_out, (char*)d_ws);
  report_k<<<1, 256, 0, stream>>>((const char*)d_ws);
}
```

```cpp
#include <hip/hip_runtime.h>
#include <hip/hip_bf16.h>
#include <stdint.h>

typedef unsigned short u16;
typedef unsigned int u32;
typedef short v4s __attribute__((ext_vector_type(4)));
typedef __bf16 v8bf __attribute__((ext_vector_type(8)));
typedef __bf16 v2bf __attribute__((ext_vector_type(2)));
typedef float v4f __attribute__((ext_vector_type(4)));
typedef float v2f __attribute__((ext_vector_type(2)));
typedef u32 v4u __attribute__((ext_vector_type(4)));
typedef u32 v2u __attribute__((ext_vector_type(2)));
#define LAS __attribute__((address_space(3)))
typedef __amdgpu_buffer_rsrc_t rsrc_t;

#define KV_WIN 24
#define KV_DELAY 0
#define W_AUX 0
#define WARM 0
#define SL1 2
#define DL2 8
#define SL2 8
#define SL3 4
#define SL4 2
#define SL5 2
#define DL5 6
#define STR2(x) #x
#define STR(x) STR2(x)

constexpr u32 WS_EPOCH = 0;
constexpr u32 WS_Q1 = 4096;
constexpr u32 WS_QA = WS_Q1 + 65536;
constexpr u32 WS_ML = WS_QA + 65536;
constexpr u32 WS_CX = WS_ML + 32768;
constexpr u32 WS_F = WS_CX + 131072;
constexpr u32 WS_OP = WS_F + 80 * 16384;
constexpr u32 WS_END = WS_OP + 256 * 16384;
constexpr u32 WS_STAMP = WS_END;

constexpr u32 RS = 1040;
constexpr u32 L_KV = 0;
constexpr u32 L_QA = 128 * RS;
constexpr u32 L_P = L_QA + 17408;
constexpr u32 L_MX = L_P + 4096;
constexpr u32 L_LS = L_MX + 512;
constexpr u32 L_QR = L_LS + 512;
constexpr u32 L_HR = L_QR + 640;
constexpr u32 L_OA = L_HR + 192;
constexpr u32 L_CNT = L_OA + 16 * 144;
constexpr u32 L_TOTAL = L_CNT + 64;
constexpr u32 UVS = 272;
static_assert(64 * UVS <= 17408, "uv image");
static_assert(L_TOTAL <= 163840, "lds");

constexpr u32 SENT = 0xFFFFFFFFu;
constexpr u32 SPIN_CAP = 1u << 18;

__device__ __forceinline__ float u2f(u32 u) { return __uint_as_float(u); }
__device__ __forceinline__ u32 f2u(float f) { return __float_as_uint(f); }
__device__ __forceinline__ float lo2f(u32 u) { return __uint_as_float(u << 16); }
__device__ __forceinline__ float hi2f(u32 u) { return __uint_as_float(u & 0xFFFF0000u); }
__device__ __forceinline__ u32 pk2(float a, float b) { v2f f = {a, b}; v2bf c = __builtin_convertvector(f, v2bf); return *(u32*)&c; }
__device__ __forceinline__ float rbf(float a) { return lo2f(pk2(a, a)); }
__device__ __forceinline__ u32 canon(u32 u) { return u < 0xFFFFFFFEu ? u : 0xFFFFFFFEu; }
__device__ __forceinline__ u32 max3u(u32 a, u32 b, u32 c) { u32 m = a > b ? a : b; return m > c ? m : c; }
__device__ __forceinline__ u32 max4u(v4u v) { return max3u(max3u(v[0], v[1], v[2]), v[3], v[3]); }
template <int CTRL> __device__ __forceinline__ float dpp(float v) { return u2f(__builtin_amdgcn_mov_dpp(f2u(v), CTRL, 0xF, 0xF, true)); }
constexpr int DPP_X1 = 0xB1, DPP_X2 = 0x4E, DPP_ROR4 = 0x124, DPP_ROR8 = 0x128, DPP_HMIR = 0x141;
__device__ __forceinline__ float sum16(float v) { v += dpp<DPP_X1>(v); v += dpp<DPP_X2>(v); v += dpp<DPP_ROR4>(v); v += dpp<DPP_ROR8>(v); return v; }
__device__ __forceinline__ float max16(float v) { v = fmaxf(v, dpp<DPP_X1>(v)); v = fmaxf(v, dpp<DPP_X2>(v)); v = fmaxf(v, dpp<DPP_ROR4>(v)); v = fmaxf(v, dpp<DPP_ROR8>(v)); return v; }
__device__ __forceinline__ float tr16(float a, float b) { auto r = __builtin_amdgcn_permlane16_swap(f2u(a), f2u(b), false, false); u32 x = r[0], y = r[1]; return u2f(x) + u2f(y); }
__device__ __forceinline__ float tr32(float a, float b) { auto r = __builtin_amdgcn_permlane32_swap(f2u(a), f2u(b), false, false); u32 x = r[0], y = r[1]; return u2f(x) + u2f(y); }
__device__ __forceinline__ float mx16(float a) { auto r = __builtin_amdgcn_permlane16_swap(f2u(a), f2u(a), false, false); u32 x = r[0], y = r[1]; return fmaxf(u2f(x), u2f(y)); }
__device__ __forceinline__ float mx32(float a) { auto r = __builtin_amdgcn_permlane32_swap(f2u(a), f2u(a), false, false); u32 x = r[0], y = r[1]; return fmaxf(u2f(x), u2f(y)); }
__device__ __forceinline__ float sum64(float v) { v = sum16(v); v = tr16(v, v); return tr32(v, v); }
__device__ __forceinline__ float max64(float v) { v = max16(v); v = mx16(v); return mx32(v); }
__device__ __forceinline__ float dot2(u32 a, u32 b, float c) { return __builtin_amdgcn_fdot2_f32_bf16(*(v2bf*)&a, *(v2bf*)&b, c, false); }
__device__ __forceinline__ v4f mfma(v4u a, v4u b, v4f c) { return __builtin_amdgcn_mfma_f32_16x16x32_bf16(*(v8bf*)&a, *(v8bf*)&b, c, 0, 0, 0); }
__device__ __forceinline__ rsrc_t mk(const void* p, u32 n) { return __builtin_amdgcn_make_buffer_rsrc((void*)p, 0, n, 0x00020000); }
__device__ __forceinline__ v4u ldw(rsrc_t r, u32 v, u32 s) { return __builtin_amdgcn_raw_buffer_load_b128(r, v, s, W_AUX); }
__device__ __forceinline__ v4u ldc(rsrc_t r, u32 v, u32 s) { return __builtin_amdgcn_raw_buffer_load_b128(r, v, s, 16); }
__device__ __forceinline__ v2u ldc2(rsrc_t r, u32 v, u32 s) { return __builtin_amdgcn_raw_buffer_load_b64(r, v, s, 16); }
__device__ __forceinline__ void stc(v4u d, rsrc_t r, u32 v, u32 s) { __builtin_amdgcn_raw_buffer_store_b128(d, r, v, s, 16); }
__device__ __forceinline__ void stc2(v2u d, rsrc_t r, u32 v, u32 s) { __builtin_amdgcn_raw_buffer_store_b64(d, r, v, s, 16); }
__device__ __forceinline__ u16 ldx(rsrc_t r, u32 elem) { return __builtin_amdgcn_raw_buffer_load_b16(r, elem * 2, 0, 0); }
#define BAR() asm volatile("s_waitcnt lgkmcnt(0)\n\ts_barrier" ::: "memory")
#define SCHED() __builtin_amdgcn_sched_barrier(0)
#define CLOB() asm volatile("" ::: "memory")
#define VM0() asm volatile("s_waitcnt vmcnt(0)" ::: "memory")
template <typename T> __device__ __forceinline__ LAS T* lds(u32 off) { return (LAS T*)(uintptr_t)off; }
__device__ __forceinline__ void fma8(float* a, v4u w, float x) {
#pragma unroll
  for (int e = 0; e < 4; ++e) { a[2 * e] = fmaf(lo2f(w[e]), x, a[2 * e]); a[2 * e + 1] = fmaf(hi2f(w[e]), x, a[2 * e + 1]); }
}
__device__ __forceinline__ void red8(const float* a, float& o0, float& o1) {
  float t0 = tr32(a[0], a[4]), t1 = tr32(a[1], a[5]), t2 = tr32(a[2], a[6]), t3 = tr32(a[3], a[7]);
  o0 = tr16(t0, t2); o1 = tr16(t1, t3);
}

#define STAMP(k) do {} while (0)

__global__ void __launch_bounds__(512, 2)
mla_ffn_kernel(const u16* x, const u16* ckv, const u16* wq, const u16* wuk,
               const u16* wuv, const u16* wo, const u16* w1, const u16* w2,
               u16* out, char* ws) {
  extern __shared__ __attribute__((aligned(16))) char smem_[];
  STAMP(0);
  asm volatile("" :: "s"(x), "s"(ckv), "s"(wq), "s"(wuk), "s"(wuv), "s"(wo), "s"(w1), "s"(w2), "s"(out), "s"(ws));
  STAMP(14);
  const u32 bi = blockIdx.x, xc = bi & 7, r = bi >> 3;
  const u32 type = r & 1;
  const u32 chunk = xc * 16 + (r >> 1);
  const u32 h = (1 - type) * 16 + xc * 2 + (r >> 4), j = (r >> 1) & 7;
  const u32 w = __builtin_amdgcn_readfirstlane(threadIdx.x >> 6), l = threadIdx.x & 63;
  const u32 l15 = l & 15, g = l >> 4;
  const rsrc_t rws = mk(ws, WS_END);
  const rsrc_t rx = mk(x, 1280);
  u32 tag;
  float mean_b = 0.f, sd_b = 1.f;
  if (threadIdx.x == 0) { *lds<v2u>(L_CNT) = (v2u){0u, 0u}; }

  if (w < 5) {
    tag = __builtin_amdgcn_raw_buffer_load_b32(rws, 0, WS_EPOCH, 16);
    u32 xs[5];
#pragma unroll
    for (int k = 0; k < 5; ++k) xs[k] = __builtin_amdgcn_raw_buffer_load_b32(rx, l * 4 + k * 256, 0, 0);
    const u32 m0 = 320 * (j & 1) + 64 * w + (l >> 2);
    u16 xr[4];
#pragma unroll
    for (int t = 0; t < 4; ++t) xr[t] = ldx(rx, m0 + 16 * t);
    const rsrc_t rq = mk(wq, 640 * 4096 * 2);
    v4u qv[4];
#pragma unroll
    for (int t = 0; t < 4; ++t) qv[t] = ldw(rq, m0 * 8192 + (l & 3) * 16 + t * 16 * 8192, h * 256 + (j >> 1) * 64);
    const rsrc_t rk = mk(wuk, w < 4 ? 512 * 4096 * 2 : 0);
    v4u kv[4];
#pragma unroll
    for (int t = 0; t < 4; ++t) kv[t] = ldw(rk, (4 * g + t) * 8192 + l15 * 16, (64 * j + 16 * w) * 8192 + h * 256);
    SCHED();
    BAR();
    SCHED();
    float s = 0.f;
#pragma unroll
    for (int k = 0; k < 5; ++k) s += lo2f(xs[k]) + hi2f(xs[k]);
    const float mean = sum64(s) * (1.f / 640.f);
    float v = 0.f;
#pragma unroll
    for (int k = 0; k < 5; ++k) { float a = lo2f(xs[k]) - mean, b = hi2f(xs[k]) - mean; v = fmaf(a, a, v); v = fmaf(b, b, v); }
    v = rbf(sum64(v) * (1.f / 640.f));
    mean_b = rbf(mean);
    sd_b = rbf(sqrtf(rbf(v + 1e-5f)));
    STAMP(1);
    float a[8] = {0.f, 0.f, 0.f, 0.f, 0.f, 0.f, 0.f, 0.f};
#pragma unroll
    for (int t = 0; t < 4; ++t) fma8(a, qv[t], rbf(rbf(lo2f(xr[t]) - mean_b) / sd_b));
    float o0, o1;
    red8(a, o0, o1);
    o0 += dpp<DPP_ROR4>(o0); o1 += dpp<DPP_ROR4>(o1);
    o0 += dpp<DPP_ROR8>(o0); o1 += dpp<DPP_ROR8>(o1);
    if (l15 < 4) *lds<v2f>(L_QR + w * 128 + (l15 * 8 + g * 2) * 4) = (v2f){o0, o1};
    asm volatile("s_waitcnt lgkmcnt(0)" ::: "memory");
    asm volatile("" : "+v"(tag));
    tag += 1u;
    u32 arrived = 0;
    if (l == 0) arrived = __hip_atomic_fetch_add(lds<u32>(L_CNT), 1u, __ATOMIC_RELAXED, __HIP_MEMORY_SCOPE_WORKGROUP);
    arrived = __builtin_amdgcn_readfirstlane(arrived);
    if (arrived == 6) {
      CLOB();
      if (l < 16) {
        v2f sacc = *lds<v2f>(L_QR + l * 8);
#pragma unroll
        for (int u = 1; u < 5; ++u) sacc += *lds<v2f>(L_QR + u * 128 + l * 8);
        stc((v4u){f2u(sacc[0]), tag, f2u(sacc[1]), tag}, rws, l * 16, WS_Q1 + h * 2048 + (j & 1) * 1024 + (j >> 1) * 256);
      }
    }
    STAMP(2);
    if (w < 4) {
      v4u p0, p1;
      _Pragma("unroll 1") for (u32 it = 0; it < SPIN_CAP; ++it) {
        CLOB();
        p0 = ldc(rws, (l15 * 4 + g) * 16, WS_Q1 + h * 2048);
        p1 = ldc(rws, (l15 * 4 + g) * 16 + 1024, WS_Q1 + h * 2048);
        const bool ok = ((p0[1] ^ tag) | (p0[3] ^ tag) | (p1[1] ^ tag) | (p1[3] ^ tag)) == 0;
        if (__all(ok)) break;
        __builtin_amdgcn_s_sleep(SL1);
      }
      STAMP(3);
      const u32 qp = pk2(u2f(p0[0]) + u2f(p1[0]), u2f(p0[2]) + u2f(p1[2]));
      auto s16 = __builtin_amdgcn_permlane16_swap(qp, qp, false, false);
      const u32 e02 = s16[0], e13 = s16[1];
      auto sa = __builtin_amdgcn_permlane32_swap(e02, e02, false, false);
      auto sb = __builtin_amdgcn_permlane32_swap(e13, e13, false, false);
      const u32 q0 = sa[0], q2 = sa[1], q1 = sb[0], q3 = sb[1];
      float d[4];
#pragma unroll
      for (int t = 0; t < 4; ++t) {
        float acc = dot2(kv[t][0], q0, 0.f);
        acc = dot2(kv[t][1], q1, acc); acc = dot2(kv[t][2], q2, acc); acc = dot2(kv[t][3], q3, acc);
        d[t] = sum16(acc) * (0.08838834764831845f * 1.4426950408889634f);
      }
      if (l15 == 0) stc((v4u){pk2(d[0], d[1]), tag, pk2(d[2], d[3]), tag}, rws, g * 16, WS_QA + h * 2048 + j * 256 + w * 64);
      if (l == 0) __hip_atomic_fetch_add(lds<u32>(L_CNT + 4), 1u, __ATOMIC_RELAXED, __HIP_MEMORY_SCOPE_WORKGROUP);
      STAMP(4);
    }
  } else if (w < 7) {
    const v4u sv = {SENT, SENT, SENT, SENT};
    tag = __builtin_amdgcn_raw_buffer_load_b32(rws, 0, WS_EPOCH, 16);
    const u32 t0 = (w - 5) * 64 + l;
    BAR();
#pragma unroll
    for (int k = 0; k < 8; ++k) stc(sv, rws, t0 * 16 + k * 2048, WS_OP + (h * 8 + j) * 16384);
#pragma unroll
    for (int k = 0; k < 2; ++k) stc(sv, rws, t0 * 16 + k * 2048, WS_F + bi * 5120);
    if (w == 5) stc(sv, rws, l * 16 + 4096, WS_F + bi * 5120);
    else if (l < 32) stc(sv, rws, (l >> 2) * 512 + (l & 3) * 16, WS_CX + h * 4096 + j * 64);
    else if (l < 40) stc(sv, rws, (l - 32) * 16, WS_ML + h * 1024 + j * 128);
    VM0();
    asm volatile("" : "+v"(tag));
    tag += 1u;
    u32 arrived = 0;
    if (l == 0) arrived = __hip_atomic_fetch_add(lds<u32>(L_CNT), 1u, __ATOMIC_RELAXED, __HIP_MEMORY_SCOPE_WORKGROUP);
    arrived = __builtin_amdgcn_readfirstlane(arrived);
    if (arrived == 6) {
      CLOB();
      if (l < 16) {
        v2f sacc = *lds<v2f>(L_QR + l * 8);
#pragma unroll
        for (int u = 1; u < 5; ++u) sacc += *lds<v2f>(L_QR + u * 128 + l * 8);
        stc((v4u){f2u(sacc[0]), tag, f2u(sacc[1]), tag}, rws, l * 16, WS_Q1 + h * 2048 + (j & 1) * 1024 + (j >> 1) * 256);
      }
    }
    STAMP(2);
  } else {
    tag = __builtin_amdgcn_raw_buffer_load_b32(rws, 0, WS_EPOCH, 16);
    BAR();
    asm volatile("" : "+v"(tag));
    tag += 1u;
    STAMP(1);
    __builtin_amdgcn_s_sleep(KV_DELAY);
    const char* src = (const char*)ckv + (size_t)chunk * 131072 + l * 16;
    u32 row = type * 64;
#pragma unroll 1
    for (u32 n = 0; n < 128; ++n) {
      u32 keep;
      asm volatile("s_waitcnt vmcnt(" STR(KV_WIN) ")\n\ts_mov_b32 %0, m0\n\ts_mov_b32 m0, %2\n\ts_nop 0\n\tglobal_load_lds_dwordx4 %1, off\n\ts_mov_b32 m0, %0"
                   : "=&s"(keep) : "v"(src + row * 1024), "s"(row * RS) : "memory");
      row = (row + 1) & 127;
    }
    STAMP(3);
  }

  _Pragma("unroll 1") for (u32 it = 0; it < SPIN_CAP * 16; ++it) {
    if (__hip_atomic_load(lds<u32>(L_CNT + 4), __ATOMIC_RELAXED, __HIP_MEMORY_SCOPE_WORKGROUP) != 0) break;
    __builtin_amdgcn_s_sleep(2);
  }
  __builtin_amdgcn_s_sleep(DL2);
  STAMP(15);
  {
    v4u p[4];
    _Pragma("unroll 1") for (u32 it = 0; it < SPIN_CAP; ++it) {
      CLOB();
      u32 bad = 0;
#pragma unroll
      for (int k = 0; k < 4; ++k) p[k] = ldc(rws, l * 16 + k * 1024, WS_QA + type * 32768 + w * 4096);
#pragma unroll
      for (int k = 0; k < 4; ++k) bad |= (p[k][1] ^ tag) | (p[k][3] ^ tag);
      if (__all(bad == 0)) break;
      __builtin_amdgcn_s_sleep(SL2);
    }
#pragma unroll
    for (int k = 0; k < 4; ++k) *lds<v2u>(L_QA + (2 * w + (k >> 1)) * RS + (k & 1) * 512 + l * 8) = (v2u){p[k][0], p[k][2]};
  }
  STAMP(5);
  VM0();
  const bool ffn = (r < 27) && (w < 5);
  const u32 f0 = 216 * xc + 8 * r;
  v4u uv[2], a1[2];
  u16 x1[2];
  {
    const rsrc_t ru = mk(wuv, 512 * 4096 * 2);
#pragma unroll
    for (int t = 0; t < 2; ++t) {
      const u32 k = 8 * w + 4 * t + g;
      const u32 G = w & 3, a = w >> 2, sg = ((G & 1) << 1) | (G >> 1);
      const u32 c = 8 * ((j >> 2) * 32 + (j & 3) * 2 + a + 8 * sg) + ((k & 7) ^ ((G & 1) << 2));
      uv[t] = ldw(ru, c * 8192 + l15 * 16, h * 256);
    }
    const rsrc_t r1 = mk(w1, ffn ? 640 * 1728 * 2 : 0);
#pragma unroll
    for (int t = 0; t < 2; ++t) { a1[t] = ldw(r1, (128 * w + 64 * t + l) * 3456, f0 * 2); x1[t] = ldx(rx, 128 * w + 64 * t + l); }
  }
  BAR();
  STAMP(6);

  v4f sacc = {0.f, 0.f, 0.f, 0.f};
  {
    const u32 ab = L_KV + (16 * w + l15) * RS + g * 256, bb = L_QA + l15 * RS + g * 256;
#pragma unroll
    for (int s = 0; s < 16; ++s) sacc = mfma(*lds<v4u>(ab + s * 16), *lds<v4u>(bb + s * 16), sacc);
  }
  asm volatile("" : "+v"(sacc));
  STAMP(16);
  v2u tv[2][2][4][2];
  {
    const u32 q = l15 >> 2, p = l & 3, sg = ((p & 1) << 1) | (p >> 1);
    const u32 base = L_KV + (4 * g + q) * RS + ((w >> 2) * 32 + (w & 3) * 2 + 8 * sg) * 16;
#pragma unroll
    for (int a = 0; a < 2; ++a)
#pragma unroll
      for (int T = 0; T < 2; ++T)
#pragma unroll
        for (int s = 0; s < 4; ++s)
#pragma unroll
          for (int t = 0; t < 2; ++t) {
            v4s rv = __builtin_amdgcn_ds_read_tr16_b64_v4i16(lds<v4s>(base + 8 * ((p & 1) ^ T) + (32 * s + 16 * t) * RS + a * 16));
            tv[a][T][s][t] = *(v2u*)&rv;
          }
  }
  float mx = fmaxf(fmaxf(sacc[0], sacc[1]), fmaxf(sacc[2], sacc[3]));
  mx = mx32(mx16(mx));
  if (g == 0) *lds<float>(L_MX + l15 * 32 + w * 4) = mx;
  BAR();
  STAMP(17);
  {
    v4f m0 = *lds<v4f>(L_MX + l15 * 32), m1 = *lds<v4f>(L_MX + l15 * 32 + 16);
    mx = fmaxf(fmaxf(fmaxf(m0[0], m0[1]), fmaxf(m0[2], m0[3])), fmaxf(fmaxf(m1[0], m1[1]), fmaxf(m1[2], m1[3])));
  }
  {
    const float e0 = __builtin_amdgcn_exp2f(sacc[0] - mx), e1 = __builtin_amdgcn_exp2f(sacc[1] - mx);
    const float e2 = __builtin_amdgcn_exp2f(sacc[2] - mx), e3 = __builtin_amdgcn_exp2f(sacc[3] - mx);
    *lds<v2u>(L_P + (w >> 1) * 1024 + l * 16 + (w & 1) * 8) = (v2u){pk2(e0, e1), pk2(e2, e3)};
    float ls = (e0 + e1) + (e2 + e3);
    ls = tr16(ls, ls); ls = tr32(ls, ls);
    if (g == 0) *lds<float>(L_LS + l15 * 32 + w * 4) = ls;
  }
  BAR();
  STAMP(7);
  {
    v4u pf[4];
#pragma unroll
    for (int s = 0; s < 4; ++s) pf[s] = *lds<v4u>(L_P + s * 1024 + l * 16);
    if (w == 0) {
      v4f a = *lds<v4f>(L_LS + l15 * 32), b = *lds<v4f>(L_LS + l15 * 32 + 16);
      const float lt = ((a[0] + a[1]) + (a[2] + a[3])) + ((b[0] + b[1]) + (b[2] + b[3]));
      if (g == 0) stc2((v2u){canon(f2u(mx)), canon(f2u(lt))}, rws, l15 * 1024, WS_ML + type * 16384 + chunk * 8);
    }
#pragma unroll
    for (int a = 0; a < 2; ++a) {
      v4f o[2];
#pragma unroll
      for (int T = 0; T < 2; ++T) {
        o[T] = (v4f){0.f, 0.f, 0.f, 0.f};
#pragma unroll
        for (int s = 0; s < 4; ++s) {
          v4u av = {tv[a][T][s][0][0], tv[a][T][s][0][1], tv[a][T][s][1][0], tv[a][T][s][1][1]};
          o[T] = mfma(av, pf[s], o[T]);
        }
      }
      v4u ov = {canon(pk2(o[0][0], o[0][1])), canon(pk2(o[0][2], o[0][3])), canon(pk2(o[1][0], o[1][1])), canon(pk2(o[1][2], o[1][3]))};
      stc(ov, rws, l15 * 131072 + g * 16 + a * 64, WS_OP + (type * 128 + w) * 16384 + chunk * 128);
    }
  }
  STAMP(8);
#pragma unroll
  for (int t = 0; t < 2; ++t) *lds<v4u>(L_QA + (8 * w + 4 * t + g) * UVS + l15 * 16) = uv[t];
  v4u ow[4], a2[2];
  {
    const rsrc_t ro = mk(wo, w < 5 ? 4096 * 640 * 2 : 0);
#pragma unroll
    for (int t = 0; t < 4; ++t) ow[t] = ldw(ro, (4 * g + t) * 1280 + l15 * 16, (h * 128 + j * 16) * 1280 + w * 256);
    const rsrc_t r2 = mk(w2, ffn ? 1728 * 640 * 2 : 0);
#pragma unroll
    for (int t = 0; t < 2; ++t) a2[t] = ldw(r2, (2 * g + t) * 1280 + l15 * 16, f0 * 1280 + w * 256);
  }
  if (w < 5) {
    float a[8] = {0.f, 0.f, 0.f, 0.f, 0.f, 0.f, 0.f, 0.f};
#pragma unroll
    for (int t = 0; t < 2; ++t) fma8(a, a1[t], rbf(rbf(lo2f(x1[t]) - mean_b) / sd_b));
    float o0, o1;
    red8(a, o0, o1);
    o0 = sum16(o0); o1 = sum16(o1);
    if (l15 == 0) *lds<v2f>(L_HR + w * 32 + g * 8) = (v2f){o0, o1};
  }
  STAMP(9);
  {
    v4u op[2], mlall; v2u mlo[2];
    _Pragma("unroll 1") for (u32 it = 0; it < SPIN_CAP; ++it) {
      CLOB();
      mlall = ldc(rws, l * 16, WS_ML + h * 1024);
#pragma unroll
      for (int t = 0; t < 2; ++t) mlo[t] = ldc2(rws, (l >> 3) * 8 + t * 64, WS_ML + h * 1024 + w * 128);
#pragma unroll
      for (int t = 0; t < 2; ++t) op[t] = ldc(rws, l * 16 + t * 1024, WS_OP + (h * 8 + j) * 16384 + w * 2048);
      const u32 mm = max3u(max3u(max4u(mlall), max4u(op[0]), max4u(op[1])), mlo[0][0], mlo[1][0]);
      if (__all(mm != SENT)) break;
      __builtin_amdgcn_s_sleep(SL3);
    }
    STAMP(10);
    const float M = max64(fmaxf(u2f(mlall[0]), u2f(mlall[2])));
    const float L = sum64(__builtin_amdgcn_exp2f(u2f(mlall[0]) - M) * u2f(mlall[1]) + __builtin_amdgcn_exp2f(u2f(mlall[2]) - M) * u2f(mlall[3]));
    float a[8] = {0.f, 0.f, 0.f, 0.f, 0.f, 0.f, 0.f, 0.f};
#pragma unroll
    for (int t = 0; t < 2; ++t) fma8(a, op[t], __builtin_amdgcn_exp2f(u2f(mlo[t][0]) - M));
    float o0, o1;
    red8(a, o0, o1);
    o0 += dpp<DPP_ROR8>(o0); o1 += dpp<DPP_ROR8>(o1);
    const float inv = 1.f / L;
    o0 *= inv; o1 *= inv;
    const u32 hi = pk2(o0, o1), lo = pk2(o0 - lo2f(hi), o1 - hi2f(hi));
    *lds<u32>(L_OA + (2 * w + ((l >> 3) & 1)) * 144 + ((l & 7) * 8 + g * 2) * 2) = (l & 8) ? lo : hi;
  }
  STAMP(18);
  BAR();
  STAMP(19);
  {
    v4f c = {0.f, 0.f, 0.f, 0.f};
#pragma unroll
    for (int s = 0; s < 2; ++s) {
      const u32 ta = L_QA + (32 * s + 8 * g + (l15 >> 2)) * UVS + w * 32 + (l & 3) * 8;
      v4s r0 = __builtin_amdgcn_ds_read_tr16_b64_v4i16(lds<v4s>(ta));
      v4s r1 = __builtin_amdgcn_ds_read_tr16_b64_v4i16(lds<v4s>(ta + 4 * UVS));
      v2u b0 = *(v2u*)&r0, b1 = *(v2u*)&r1;
      c = mfma(*lds<v4u>(L_OA + l15 * 144 + s * 64 + g * 16), (v4u){b0[0], b0[1], b1[0], b1[1]}, c);
    }
    float cs = (c[0] + c[1]) + (c[2] + c[3]);
    cs = tr16(cs, cs); cs = tr32(cs, cs);
    const u32 cu = canon(f2u(cs));
    const v4u cv = {(u32)__builtin_amdgcn_mov_dpp(cu, 0x00, 0xF, 0xF, true), (u32)__builtin_amdgcn_mov_dpp(cu, 0x55, 0xF, 0xF, true),
                    (u32)__builtin_amdgcn_mov_dpp(cu, 0xAA, 0xF, 0xF, true), (u32)__builtin_amdgcn_mov_dpp(cu, 0xFF, 0xF, 0xF, true)};
    if (l < 16 && (l & 3) == 0) stc(cv, rws, l * 4, WS_CX + h * 4096 + w * 512 + j * 64);
  }
  STAMP(11);
  if (w < 5) {
    float y0, y1;
    {
      float hv[2];
      v2f hs = *lds<v2f>(L_HR + g * 8);
#pragma unroll
      for (int u = 1; u < 5; ++u) hs += *lds<v2f>(L_HR + u * 32 + g * 8);
      hv[0] = fmaxf(rbf(hs[0]), 0.f); hv[1] = fmaxf(rbf(hs[1]), 0.f);
      float a[8] = {0.f, 0.f, 0.f, 0.f, 0.f, 0.f, 0.f, 0.f};
#pragma unroll
      for (int t = 0; t < 2; ++t) fma8(a, a2[t], hv[t]);
      red8(a, y0, y1);
    }
    v4u cx;
    _Pragma("unroll 1") for (u32 it = 0; it < SPIN_CAP; ++it) {
      CLOB();
      cx = ldc(rws, (l & 7) * 64 + g * 16, WS_CX + h * 4096 + j * 512);
      if (__all(max4u(cx) != SENT)) break;
      __builtin_amdgcn_s_sleep(SL4);
    }
    STAMP(12);
    float a[8] = {0.f, 0.f, 0.f, 0.f, 0.f, 0.f, 0.f, 0.f};
#pragma unroll
    for (int t = 0; t < 4; ++t) {
      float c = u2f(cx[t]);
      c += dpp<DPP_X1>(c); c += dpp<DPP_X2>(c); c += dpp<DPP_HMIR>(c);
      fma8(a, ow[t], rbf(c));
    }
    float s0, s1;
    red8(a, s0, s1);
    stc((v4u){canon(f2u(s0)), canon(f2u(y0)), canon(f2u(s1)), canon(f2u(y1))}, rws, l15 * 16384 + g * 16, WS_F + w * 16 * 16384 + bi * 64);
    if (l == 0) __hip_atomic_store(lds<u32>(L_CNT + 4), 100u, __ATOMIC_RELAXED, __HIP_MEMORY_SCOPE_WORKGROUP);
    STAMP(13);
  } else if (w < 7 && bi < 160) {
    const u32 rid = 2 * bi + (w - 5);
    const u32 xv = __builtin_amdgcn_raw_buffer_load_b32(rx, rid * 4, 0, 0);
    _Pragma("unroll 1") for (u32 it = 0; it < SPIN_CAP * 16; ++it) {
      if (__hip_atomic_load(lds<u32>(L_CNT + 4), __ATOMIC_RELAXED, __HIP_MEMORY_SCOPE_WORKGROUP) == 100u) break;
      __builtin_amdgcn_s_sleep(1);
    }
    __builtin_amdgcn_s_sleep(DL5);
    v4u f[4];
    _Pragma("unroll 1") for (u32 it = 0; it < SPIN_CAP; ++it) {
      CLOB();
#pragma unroll
      for (int t = 0; t < 4; ++t) f[t] = ldc(rws, l * 64 + t * 4096, WS_F + (rid >> 2) * 16384 + (rid & 3) * 16);
      if (__all(max3u(max3u(max4u(f[0]), max4u(f[1]), max4u(f[2])), max4u(f[3]), 0u) != SENT)) break;
      __builtin_amdgcn_s_sleep(SL5);
    }
    STAMP(12);
    float v[4];
#pragma unroll
    for (int e = 0; e < 4; ++e) v[e] = (u2f(f[0][e]) + u2f(f[1][e])) + (u2f(f[2][e]) + u2f(f[3][e]));
    float sv = tr32(v[0], v[2]), yv = tr32(v[1], v[3]);
    sv = sum16(sv); yv = sum16(yv);
    sv = rbf(tr16(sv, sv)); yv = rbf(tr16(yv, yv));
    const float gt = rbf(yv / (1.f + __builtin_amdgcn_exp2f(-1.4426950408889634f * yv)));
    const float xx = (l < 32) ? lo2f(xv) : hi2f(xv);
    const u32 ob = pk2(xx + rbf(sv * gt), 0.f);
    if ((l & 31) == 0) out[2 * rid + (l >> 5)] = (u16)ob;
    if (rid == 0 && l == 0) __builtin_amdgcn_raw_buffer_store_b32(tag, rws, 0, WS_EPOCH, 16);
    STAMP(13);
  }
}

extern "C" void kernel_launch(void* const* d_in, const int* in_sizes, int n_in, void* d_out, int out_size, void* d_ws, size_t ws_size,
                              hipStream_t stream) {
  (void)in_sizes; (void)n_in; (void)out_size; (void)ws_size;
  (void)hipFuncSetAttribute((const void*)mla_ffn_kernel, hipFuncAttributeMaxDynamicSharedMemorySize, L_TOTAL);
  mla_ffn_kernel<<<256, 512, L_TOTAL, stream>>>((const u16*)d_in[0], (const u16*)d_in[1], (const u16*)d_in[2], (const u16*)d_in[3],
                                                (const u16*)d_in[4], (const u16*)d_in[5], (const u16*)d_in[7], (const u16*)d_in[8],
                                                (u16*)d_out, (char*)d_ws);
}
```

```cpp
#define STAMPS
#include <hip/hip_runtime.h>
#include <hip/hip_bf16.h>
#include <stdint.h>

typedef unsigned short u16;
typedef unsigned int u32;
typedef short v4s __attribute__((ext_vector_type(4)));
typedef __bf16 v8bf __attribute__((ext_vector_type(8)));
typedef __bf16 v2bf __attribute__((ext_vector_type(2)));
typedef float v4f __attribute__((ext_vector_type(4)));
typedef float v2f __attribute__((ext_vector_type(2)));
typedef u32 v4u __attribute__((ext_vector_type(4)));
typedef u32 v2u __attribute__((ext_vector_type(2)));
#define LAS __attribute__((address_space(3)))
typedef __amdgpu_buffer_rsrc_t rsrc_t;

#define KV_WIN 24
#define KV_DELAY 0
#define W_AUX 0
#define WARM 0
#define SL1 2
#define DL2 8
#define SL2 8
#define SL3 4
#define SL4 2
#define SL5 2
#define DL5 6
#define STR2(x) #x
#define STR(x) STR2(x)

constexpr u32 WS_EPOCH = 0;
constexpr u32 WS_Q1 = 4096;
constexpr u32 WS_QA = WS_Q1 + 65536;
constexpr u32 WS_ML = WS_QA + 65536;
constexpr u32 WS_CX = WS_ML + 32768;
constexpr u32 WS_F = WS_CX + 131072;
constexpr u32 WS_OP = WS_F + 80 * 16384;
constexpr u32 WS_END = WS_OP + 256 * 16384;
constexpr u32 WS_STAMP = WS_END;

constexpr u32 RS = 1040;
constexpr u32 L_KV = 0;
constexpr u32 L_QA = 128 * RS;
constexpr u32 L_P = L_QA + 17408;
constexpr u32 L_MX = L_P + 4096;
constexpr u32 L_LS = L_MX + 512;
constexpr u32 L_QR = L_LS + 512;
constexpr u32 L_HR = L_QR + 640;
constexpr u32 L_OA = L_HR + 192;
constexpr u32 L_CNT = L_OA + 16 * 144;
constexpr u32 L_TOTAL = L_CNT + 64;
constexpr u32 UVS = 272;
static_assert(64 * UVS <= 17408, "uv image");
static_assert(L_TOTAL <= 163840, "lds");

constexpr u32 SENT = 0xFFFFFFFFu;
constexpr u32 SPIN_CAP = 1u << 18;

__device__ __forceinline__ float u2f(u32 u) { return __uint_as_float(u); }
__device__ __forceinline__ u32 f2u(float f) { return __float_as_uint(f); }
__device__ __forceinline__ float lo2f(u32 u) { return __uint_as_float(u << 16); }
__device__ __forceinline__ float hi2f(u32 u) { return __uint_as_float(u & 0xFFFF0000u); }
__device__ __forceinline__ u32 pk2(float a, float b) { v2f f = {a, b}; v2bf c = __builtin_convertvector(f, v2bf); return *(u32*)&c; }
__device__ __forceinline__ float rbf(float a) { return lo2f(pk2(a, a)); }
__device__ __forceinline__ u32 canon(u32 u) { return u < 0xFFFFFFFEu ? u : 0xFFFFFFFEu; }
__device__ __forceinline__ u32 max3u(u32 a, u32 b, u32 c) { u32 m = a > b ? a : b; return m > c ? m : c; }
__device__ __forceinline__ u32 max4u(v4u v) { return max3u(max3u(v[0], v[1], v[2]), v[3], v[3]); }
template <int CTRL> __device__ __forceinline__ float dpp(float v) { return u2f(__builtin_amdgcn_mov_dpp(f2u(v), CTRL, 0xF, 0xF, true)); }
constexpr int DPP_X1 = 0xB1, DPP_X2 = 0x4E, DPP_ROR4 = 0x124, DPP_ROR8 = 0x128, DPP_HMIR = 0x141;
__device__ __forceinline__ float sum16(float v) { v += dpp<DPP_X1>(v); v += dpp<DPP_X2>(v); v += dpp<DPP_ROR4>(v); v += dpp<DPP_ROR8>(v); return v; }
__device__ __forceinline__ float max16(float v) { v = fmaxf(v, dpp<DPP_X1>(v)); v = fmaxf(v, dpp<DPP_X2>(v)); v = fmaxf(v, dpp<DPP_ROR4>(v)); v = fmaxf(v, dpp<DPP_ROR8>(v)); return v; }
__device__ __forceinline__ float tr16(float a, float b) { auto r = __builtin_amdgcn_permlane16_swap(f2u(a), f2u(b), false, false); u32 x = r[0], y = r[1]; return u2f(x) + u2f(y); }
__device__ __forceinline__ float tr32(float a, float b) { auto r = __builtin_amdgcn_permlane32_swap(f2u(a), f2u(b), false, false); u32 x = r[0], y = r[1]; return u2f(x) + u2f(y); }
__device__ __forceinline__ float mx16(float a) { auto r = __builtin_amdgcn_permlane16_swap(f2u(a), f2u(a), false, false); u32 x = r[0], y = r[1]; return fmaxf(u2f(x), u2f(y)); }
__device__ __forceinline__ float mx32(float a) { auto r = __builtin_amdgcn_permlane32_swap(f2u(a), f2u(a), false, false); u32 x = r[0], y = r[1]; return fmaxf(u2f(x), u2f(y)); }
__device__ __forceinline__ float sum64(float v) { v = sum16(v); v = tr16(v, v); return tr32(v, v); }
__device__ __forceinline__ float max64(float v) { v = max16(v); v = mx16(v); return mx32(v); }
__device__ __forceinline__ float dot2(u32 a, u32 b, float c) { return __builtin_amdgcn_fdot2_f32_bf16(*(v2bf*)&a, *(v2bf*)&b, c, false); }
__device__ __forceinline__ v4f mfma(v4u a, v4u b, v4f c) { return __builtin_amdgcn_mfma_f32_16x16x32_bf16(*(v8bf*)&a, *(v8bf*)&b, c, 0, 0, 0); }
__device__ __forceinline__ rsrc_t mk(const void* p, u32 n) { return __builtin_amdgcn_make_buffer_rsrc((void*)p, 0, n, 0x00020000); }
__device__ __forceinline__ v4u ldw(rsrc_t r, u32 v, u32 s) { return __builtin_amdgcn_raw_buffer_load_b128(r, v, s, W_AUX); }
__device__ __forceinline__ v4u ldc(rsrc_t r, u32 v, u32 s) { return __builtin_amdgcn_raw_buffer_load_b128(r, v, s, 16); }
__device__ __forceinline__ v2u ldc2(rsrc_t r, u32 v, u32 s) { return __builtin_amdgcn_raw_buffer_load_b64(r, v, s, 16); }
__device__ __forceinline__ void stc(v4u d, rsrc_t r, u32 v, u32 s) { __builtin_amdgcn_raw_buffer_store_b128(d, r, v, s, 16); }
__device__ __forceinline__ void stc2(v2u d, rsrc_t r, u32 v, u32 s) { __builtin_amdgcn_raw_buffer_store_b64(d, r, v, s, 16); }
__device__ __forceinline__ u16 ldx(rsrc_t r, u32 elem) { return __builtin_amdgcn_raw_buffer_load_b16(r, elem * 2, 0, 0); }
#define BAR() asm volatile("s_waitcnt lgkmcnt(0)\n\ts_barrier" ::: "memory")
#define SCHED() __builtin_amdgcn_sched_barrier(0)
#define CLOB() asm volatile("" ::: "memory")
#define VM0() asm volatile("s_waitcnt vmcnt(0)" ::: "memory")
template <typename T> __device__ __forceinline__ LAS T* lds(u32 off) { return (LAS T*)(uintptr_t)off; }
__device__ __forceinline__ void fma8(float* a, v4u w, float x) {
#pragma unroll
  for (int e = 0; e < 4; ++e) { a[2 * e] = fmaf(lo2f(w[e]), x, a[2 * e]); a[2 * e + 1] = fmaf(hi2f(w[e]), x, a[2 * e + 1]); }
}
__device__ __forceinline__ void red8(const float* a, float& o0, float& o1) {
  float t0 = tr32(a[0], a[4]), t1 = tr32(a[1], a[5]), t2 = tr32(a[2], a[6]), t3 = tr32(a[3], a[7]);
  o0 = tr16(t0, t2); o1 = tr16(t1, t3);
}

#define NST 24
#define STAMP(k) do { st[k] = (u32)__builtin_amdgcn_s_memrealtime(); } while (0)

__global__ void __launch_bounds__(512, 2)
mla_ffn_kernel(const u16* x, const u16* ckv, const u16* wq, const u16* wuk,
               const u16* wuv, const u16* wo, const u16* w1, const u16* w2,
               u16* out, char* ws) {
  extern __shared__ __attribute__((aligned(16))) char smem_[];
  u32 st[NST];
#pragma unroll
  for (int k = 0; k < NST; ++k) st[k] = 0;
  STAMP(0);
  const unsigned long long clk0 = __builtin_amdgcn_s_memtime();
  asm volatile("" :: "s"(x), "s"(ckv), "s"(wq), "s"(wuk), "s"(wuv), "s"(wo), "s"(w1), "s"(w2), "s"(out), "s"(ws));
  STAMP(14);
  const u32 bi = blockIdx.x, xc = bi & 7, r = bi >> 3;
  const u32 type = r & 1;
  const u32 chunk = xc * 16 + (r >> 1);
  const u32 h = (1 - type) * 16 + xc * 2 + (r >> 4), j = (r >> 1) & 7;
  const u32 w = __builtin_amdgcn_readfirstlane(threadIdx.x >> 6), l = threadIdx.x & 63;
  const u32 l15 = l & 15, g = l >> 4;
  const rsrc_t rws = mk(ws, WS_END);
  const rsrc_t rx = mk(x, 1280);
  u32 tag;
  float mean_b = 0.f, sd_b = 1.f;
  if (threadIdx.x == 0) { *lds<v2u>(L_CNT) = (v2u){0u, 0u}; }

  if (w < 5) {
    tag = __builtin_amdgcn_raw_buffer_load_b32(rws, 0, WS_EPOCH, 16);
    u32 xs[5];
#pragma unroll
    for (int k = 0; k < 5; ++k) xs[k] = __builtin_amdgcn_raw_buffer_load_b32(rx, l * 4 + k * 256, 0, 0);
    const u32 m0 = 320 * (j & 1) + 64 * w + (l >> 2);
    u16 xr[4];
#pragma unroll
    for (int t = 0; t < 4; ++t) xr[t] = ldx(rx, m0 + 16 * t);
    const rsrc_t rq = mk(wq, 640 * 4096 * 2);
    v4u qv[4];
#pragma unroll
    for (int t = 0; t < 4; ++t) qv[t] = ldw(rq, m0 * 8192 + (l & 3) * 16 + t * 16 * 8192, h * 256 + (j >> 1) * 64);
    const rsrc_t rk = mk(wuk, w < 4 ? 512 * 4096 * 2 : 0);
    v4u kv[4];
#pragma unroll
    for (int t = 0; t < 4; ++t) kv[t] = ldw(rk, (4 * g + t) * 8192 + l15 * 16, (64 * j + 16 * w) * 8192 + h * 256);
    SCHED();
    BAR();
    SCHED();
    float s = 0.f;
#pragma unroll
    for (int k = 0; k < 5; ++k) s += lo2f(xs[k]) + hi2f(xs[k]);
    const float mean = sum64(s) * (1.f / 640.f);
    float v = 0.f;
#pragma unroll
    for (int k = 0; k < 5; ++k) { float a = lo2f(xs[k]) - mean, b = hi2f(xs[k]) - mean; v = fmaf(a, a, v); v = fmaf(b, b, v); }
    v = rbf(sum64(v) * (1.f / 640.f));
    mean_b = rbf(mean);
    sd_b = rbf(sqrtf(rbf(v + 1e-5f)));
    STAMP(1);
    float a[8] = {0.f, 0.f, 0.f, 0.f, 0.f, 0.f, 0.f, 0.f};
#pragma unroll
    for (int t = 0; t < 4; ++t) fma8(a, qv[t], rbf(rbf(lo2f(xr[t]) - mean_b) / sd_b));
    float o0, o1;
    red8(a, o0, o1);
    o0 += dpp<DPP_ROR4>(o0); o1 += dpp<DPP_ROR4>(o1);
    o0 += dpp<DPP_ROR8>(o0); o1 += dpp<DPP_ROR8>(o1);
    if (l15 < 4) *lds<v2f>(L_QR + w * 128 + (l15 * 8 + g * 2) * 4) = (v2f){o0, o1};
    asm volatile("s_waitcnt lgkmcnt(0)" ::: "memory");
    asm volatile("" : "+v"(tag));
    tag += 1u;
    u32 arrived = 0;
    if (l == 0) arrived = __hip_atomic_fetch_add(lds<u32>(L_CNT), 1u, __ATOMIC_RELAXED, __HIP_MEMORY_SCOPE_WORKGROUP);
    arrived = __builtin_amdgcn_readfirstlane(arrived);
    if (arrived == 6) {
      CLOB();
      if (l < 16) {
        v2f sacc = *lds<v2f>(L_QR + l * 8);
#pragma unroll
        for (int u = 1; u < 5; ++u) sacc += *lds<v2f>(L_QR + u * 128 + l * 8);
        stc((v4u){f2u(sacc[0]), tag, f2u(sacc[1]), tag}, rws, l * 16, WS_Q1 + h * 2048 + (j & 1) * 1024 + (j >> 1) * 256);
      }
    }
    STAMP(2);
    if (w < 4) {
      v4u p0, p1;
      _Pragma("unroll 1") for (u32 it = 0; it < SPIN_CAP; ++it) {
        CLOB();
        p0 = ldc(rws, (l15 * 4 + g) * 16, WS_Q1 + h * 2048);
        p1 = ldc(rws, (l15 * 4 + g) * 16 + 1024, WS_Q1 + h * 2048);
        const bool ok = ((p0[1] ^ tag) | (p0[3] ^ tag) | (p1[1] ^ tag) | (p1[3] ^ tag)) == 0;
        if (__all(ok)) break;
        __builtin_amdgcn_s_sleep(SL1);
      }
      STAMP(3);
      const u32 qp = pk2(u2f(p0[0]) + u2f(p1[0]), u2f(p0[2]) + u2f(p1[2]));
      auto s16 = __builtin_amdgcn_permlane16_swap(qp, qp, false, false);
      const u32 e02 = s16[0], e13 = s16[1];
      auto sa = __builtin_amdgcn_permlane32_swap(e02, e02, false, false);
      auto sb = __builtin_amdgcn_permlane32_swap(e13, e13, false, false);
      const u32 q0 = sa[0], q2 = sa[1], q1 = sb[0], q3 = sb[1];
      float d[4];
#pragma unroll
      for (int t = 0; t < 4; ++t) {
        float acc = dot2(kv[t][0], q0, 0.f);
        acc = dot2(kv[t][1], q1, acc); acc = dot2(kv[t][2], q2, acc); acc = dot2(kv[t][3], q3, acc);
        d[t] = sum16(acc) * (0.08838834764831845f * 1.4426950408889634f);
      }
      if (l15 == 0) stc((v4u){pk2(d[0], d[1]), tag, pk2(d[2], d[3]), tag}, rws, g * 16, WS_QA + h * 2048 + j * 256 + w * 64);
      if (l == 0) __hip_atomic_fetch_add(lds<u32>(L_CNT + 4), 1u, __ATOMIC_RELAXED, __HIP_MEMORY_SCOPE_WORKGROUP);
      STAMP(4);
    }
  } else if (w < 7) {
    const v4u sv = {SENT, SENT, SENT, SENT};
    tag = __builtin_amdgcn_raw_buffer_load_b32(rws, 0, WS_EPOCH, 16);
    const u32 t0 = (w - 5) * 64 + l;
    BAR();
#pragma unroll
    for (int k = 0; k < 8; ++k) stc(sv, rws, t0 * 16 + k * 2048, WS_OP + (h * 8 + j) * 16384);
#pragma unroll
    for (int k = 0; k < 2; ++k) stc(sv, rws, t0 * 16 + k * 2048, WS_F + bi * 5120);
    if (w == 5) stc(sv, rws, l * 16 + 4096, WS_F + bi * 5120);
    else if (l < 32) stc(sv, rws, (l >> 2) * 512 + (l & 3) * 16, WS_CX + h * 4096 + j * 64);
    else if (l < 40) stc(sv, rws, (l - 32) * 16, WS_ML + h * 1024 + j * 128);
    VM0();
    asm volatile("" : "+v"(tag));
    tag += 1u;
    u32 arrived = 0;
    if (l == 0) arrived = __hip_atomic_fetch_add(lds<u32>(L_CNT), 1u, __ATOMIC_RELAXED, __HIP_MEMORY_SCOPE_WORKGROUP);
    arrived = __builtin_amdgcn_readfirstlane(arrived);
    if (arrived == 6) {
      CLOB();
      if (l < 16) {
        v2f sacc = *lds<v2f>(L_QR + l * 8);
#pragma unroll
        for (int u = 1; u < 5; ++u) sacc += *lds<v2f>(L_QR + u * 128 + l * 8);
        stc((v4u){f2u(sacc[0]), tag, f2u(sacc[1]), tag}, rws, l * 16, WS_Q1 + h * 2048 + (j & 1) * 1024 + (j >> 1) * 256);
      }
    }
    STAMP(2);
  } else {
    tag = __builtin_amdgcn_raw_buffer_load_b32(rws, 0, WS_EPOCH, 16);
    BAR();
    asm volatile("" : "+v"(tag));
    tag += 1u;
    STAMP(1);
    __builtin_amdgcn_s_sleep(KV_DELAY);
    const char* src = (const char*)ckv + (size_t)chunk * 131072 + l * 16;
    u32 row = type * 64;
#pragma unroll 1
    for (u32 n = 0; n < 128; ++n) {
      u32 keep;
      asm volatile("s_waitcnt vmcnt(" STR(KV_WIN) ")\n\ts_mov_b32 %0, m0\n\ts_mov_b32 m0, %2\n\ts_nop 0\n\tglobal_load_lds_dwordx4 %1, off\n\ts_mov_b32 m0, %0"
                   : "=&s"(keep) : "v"(src + row * 1024), "s"(row * RS) : "memory");
      row = (row + 1) & 127;
    }
    STAMP(3);
  }

  _Pragma("unroll 1") for (u32 it = 0; it < SPIN_CAP * 16; ++it) {
    if (__hip_atomic_load(lds<u32>(L_CNT + 4), __ATOMIC_RELAXED, __HIP_MEMORY_SCOPE_WORKGROUP) != 0) break;
    __builtin_amdgcn_s_sleep(2);
  }
  __builtin_amdgcn_s_sleep(DL2);
  STAMP(15);
  {
    v4u p[4];
    _Pragma("unroll 1") for (u32 it = 0; it < SPIN_CAP; ++it) {
      CLOB();
      u32 bad = 0;
#pragma unroll
      for (int k = 0; k < 4; ++k) p[k] = ldc(rws, l * 16 + k * 1024, WS_QA + type * 32768 + w * 4096);
#pragma unroll
      for (int k = 0; k < 4; ++k) bad |= (p[k][1] ^ tag) | (p[k][3] ^ tag);
      if (__all(bad == 0)) break;
      __builtin_amdgcn_s_sleep(SL2);
    }
#pragma unroll
    for (int k = 0; k < 4; ++k) *lds<v2u>(L_QA + (2 * w + (k >> 1)) * RS + (k & 1) * 512 + l * 8) = (v2u){p[k][0], p[k][2]};
  }
  STAMP(5);
  VM0();
  const bool ffn = (r < 27) && (w < 5);
  const u32 f0 = 216 * xc + 8 * r;
  v4u uv[2], a1[2];
  u16 x1[2];
  {
    const rsrc_t ru = mk(wuv, 512 * 4096 * 2);
#pragma unroll
    for (int t = 0; t < 2; ++t) {
      const u32 k = 8 * w + 4 * t + g;
      const u32 G = w & 3, a = w >> 2, sg = ((G & 1) << 1) | (G >> 1);
      const u32 c = 8 * ((j >> 2) * 32 + (j & 3) * 2 + a + 8 * sg) + ((k & 7) ^ ((G & 1) << 2));
      uv[t] = ldw(ru, c * 8192 + l15 * 16, h * 256);
    }
    const rsrc_t r1 = mk(w1, ffn ? 640 * 1728 * 2 : 0);
#pragma unroll
    for (int t = 0; t < 2; ++t) { a1[t] = ldw(r1, (128 * w + 64 * t + l) * 3456, f0 * 2); x1[t] = ldx(rx, 128 * w + 64 * t + l); }
  }
  BAR();
  STAMP(6);

  v4f sacc = {0.f, 0.f, 0.f, 0.f};
  {
    const u32 ab = L_KV + (16 * w + l15) * RS + g * 256, bb = L_QA + l15 * RS + g * 256;
#pragma unroll
    for (int s = 0; s < 16; ++s) sacc = mfma(*lds<v4u>(ab + s * 16), *lds<v4u>(bb + s * 16), sacc);
  }
  asm volatile("" : "+v"(sacc));
  STAMP(16);
  v2u tv[2][2][4][2];
  {
    const u32 q = l15 >> 2, p = l & 3, sg = ((p & 1) << 1) | (p >> 1);
    const u32 base = L_KV + (4 * g + q) * RS + ((w >> 2) * 32 + (w & 3) * 2 + 8 * sg) * 16;
#pragma unroll
    for (int a = 0; a < 2; ++a)
#pragma unroll
      for (int T = 0; T < 2; ++T)
#pragma unroll
        for (int s = 0; s < 4; ++s)
#pragma unroll
          for (int t = 0; t < 2; ++t) {
            v4s rv = __builtin_amdgcn_ds_read_tr16_b64_v4i16(lds<v4s>(base + 8 * ((p & 1) ^ T) + (32 * s + 16 * t) * RS + a * 16));
            tv[a][T][s][t] = *(v2u*)&rv;
          }
  }
  float mx = fmaxf(fmaxf(sacc[0], sacc[1]), fmaxf(sacc[2], sacc[3]));
  mx = mx32(mx16(mx));
  if (g == 0) *lds<float>(L_MX + l15 * 32 + w * 4) = mx;
  BAR();
  STAMP(17);
  {
    v4f m0 = *lds<v4f>(L_MX + l15 * 32), m1 = *lds<v4f>(L_MX + l15 * 32 + 16);
    mx = fmaxf(fmaxf(fmaxf(m0[0], m0[1]), fmaxf(m0[2], m0[3])), fmaxf(fmaxf(m1[0], m1[1]), fmaxf(m1[2], m1[3])));
  }
  {
    const float e0 = __builtin_amdgcn_exp2f(sacc[0] - mx), e1 = __builtin_amdgcn_exp2f(sacc[1] - mx);
    const float e2 = __builtin_amdgcn_exp2f(sacc[2] - mx), e3 = __builtin_amdgcn_exp2f(sacc[3] - mx);
    *lds<v2u>(L_P + (w >> 1) * 1024 + l * 16 + (w & 1) * 8) = (v2u){pk2(e0, e1), pk2(e2, e3)};
    float ls = (e0 + e1) + (e2 + e3);
    ls = tr16(ls, ls); ls = tr32(ls, ls);
    if (g == 0) *lds<float>(L_LS + l15 * 32 + w * 4) = ls;
  }
  BAR();
  STAMP(7);
  {
    v4u pf[4];
#pragma unroll
    for (int s = 0; s < 4; ++s) pf[s] = *lds<v4u>(L_P + s * 1024 + l * 16);
    if (w == 0) {
      v4f a = *lds<v4f>(L_LS + l15 * 32), b = *lds<v4f>(L_LS + l15 * 32 + 16);
      const float lt = ((a[0] + a[1]) + (a[2] + a[3])) + ((b[0] + b[1]) + (b[2] + b[3]));
      if (g == 0) stc2((v2u){canon(f2u(mx)), canon(f2u(lt))}, rws, l15 * 1024, WS_ML + type * 16384 + chunk * 8);
    }
#pragma unroll
    for (int a = 0; a < 2; ++a) {
      v4f o[2];
#pragma unroll
      for (int T = 0; T < 2; ++T) {
        o[T] = (v4f){0.f, 0.f, 0.f, 0.f};
#pragma unroll
        for (int s = 0; s < 4; ++s) {
          v4u av = {tv[a][T][s][0][0], tv[a][T][s][0][1], tv[a][T][s][1][0], tv[a][T][s][1][1]};
          o[T] = mfma(av, pf[s], o[T]);
        }
      }
      v4u ov = {canon(pk2(o[0][0], o[0][1])), canon(pk2(o[0][2], o[0][3])), canon(pk2(o[1][0], o[1][1])), canon(pk2(o[1][2], o[1][3]))};
      stc(ov, rws, l15 * 131072 + g * 16 + a * 64, WS_OP + (type * 128 + w) * 16384 + chunk * 128);
    }
  }
  STAMP(8);
#pragma unroll
  for (int t = 0; t < 2; ++t) *lds<v4u>(L_QA + (8 * w + 4 * t + g) * UVS + l15 * 16) = uv[t];
  v4u ow[4], a2[2];
  {
    const rsrc_t ro = mk(wo, w < 5 ? 4096 * 640 * 2 : 0);
#pragma unroll
    for (int t = 0; t < 4; ++t) ow[t] = ldw(ro, (4 * g + t) * 1280 + l15 * 16, (h * 128 + j * 16) * 1280 + w * 256);
    const rsrc_t r2 = mk(w2, ffn ? 1728 * 640 * 2 : 0);
#pragma unroll
    for (int t = 0; t < 2; ++t) a2[t] = ldw(r2, (2 * g + t) * 1280 + l15 * 16, f0 * 1280 + w * 256);
  }
  if (w < 5) {
    float a[8] = {0.f, 0.f, 0.f, 0.f, 0.f, 0.f, 0.f, 0.f};
#pragma unroll
    for (int t = 0; t < 2; ++t) fma8(a, a1[t], rbf(rbf(lo2f(x1[t]) - mean_b) / sd_b));
    float o0, o1;
    red8(a, o0, o1);
    o0 = sum16(o0); o1 = sum16(o1);
    if (l15 == 0) *lds<v2f>(L_HR + w * 32 + g * 8) = (v2f){o0, o1};
  }
  STAMP(9);
  {
    v4u op[2], mlall; v2u mlo[2];
    _Pragma("unroll 1") for (u32 it = 0; it < SPIN_CAP; ++it) {
      CLOB();
      mlall = ldc(rws, l * 16, WS_ML + h * 1024);
#pragma unroll
      for (int t = 0; t < 2; ++t) mlo[t] = ldc2(rws, (l >> 3) * 8 + t * 64, WS_ML + h * 1024 + w * 128);
#pragma unroll
      for (int t = 0; t < 2; ++t) op[t] = ldc(rws, l * 16 + t * 1024, WS_OP + (h * 8 + j) * 16384 + w * 2048);
      const u32 mm = max3u(max3u(max4u(mlall), max4u(op[0]), max4u(op[1])), mlo[0][0], mlo[1][0]);
      if (__all(mm != SENT)) break;
      __builtin_amdgcn_s_sleep(SL3);
    }
    STAMP(10);
    const float M = max64(fmaxf(u2f(mlall[0]), u2f(mlall[2])));
    const float L = sum64(__builtin_amdgcn_exp2f(u2f(mlall[0]) - M) * u2f(mlall[1]) + __builtin_amdgcn_exp2f(u2f(mlall[2]) - M) * u2f(mlall[3]));
    float a[8] = {0.f, 0.f, 0.f, 0.f, 0.f, 0.f, 0.f, 0.f};
#pragma unroll
    for (int t = 0; t < 2; ++t) fma8(a, op[t], __builtin_amdgcn_exp2f(u2f(mlo[t][0]) - M));
    float o0, o1;
    red8(a, o0, o1);
    o0 += dpp<DPP_ROR8>(o0); o1 += dpp<DPP_ROR8>(o1);
    const float inv = 1.f / L;
    o0 *= inv; o1 *= inv;
    const u32 hi = pk2(o0, o1), lo = pk2(o0 - lo2f(hi), o1 - hi2f(hi));
    *lds<u32>(L_OA + (2 * w + ((l >> 3) & 1)) * 144 + ((l & 7) * 8 + g * 2) * 2) = (l & 8) ? lo : hi;
  }
  STAMP(18);
  BAR();
  STAMP(19);
  {
    v4f c = {0.f, 0.f, 0.f, 0.f};
#pragma unroll
    for (int s = 0; s < 2; ++s) {
      const u32 ta = L_QA + (32 * s + 8 * g + (l15 >> 2)) * UVS + w * 32 + (l & 3) * 8;
      v4s r0 = __builtin_amdgcn_ds_read_tr16_b64_v4i16(lds<v4s>(ta));
      v4s r1 = __builtin_amdgcn_ds_read_tr16_b64_v4i16(lds<v4s>(ta + 4 * UVS));
      v2u b0 = *(v2u*)&r0, b1 = *(v2u*)&r1;
      c = mfma(*lds<v4u>(L_OA + l15 * 144 + s * 64 + g * 16), (v4u){b0[0], b0[1], b1[0], b1[1]}, c);
    }
    float cs = (c[0] + c[1]) + (c[2] + c[3]);
    cs = tr16(cs, cs); cs = tr32(cs, cs);
    const u32 cu = canon(f2u(cs));
    const v4u cv = {(u32)__builtin_amdgcn_mov_dpp(cu, 0x00, 0xF, 0xF, true), (u32)__builtin_amdgcn_mov_dpp(cu, 0x55, 0xF, 0xF, true),
                    (u32)__builtin_amdgcn_mov_dpp(cu, 0xAA, 0xF, 0xF, true), (u32)__builtin_amdgcn_mov_dpp(cu, 0xFF, 0xF, 0xF, true)};
    if (l < 16 && (l & 3) == 0) stc(cv, rws, l * 4, WS_CX + h * 4096 + w * 512 + j * 64);
  }
  STAMP(11);
  if (w < 5) {
    float y0, y1;
    {
      float hv[2];
      v2f hs = *lds<v2f>(L_HR + g * 8);
#pragma unroll
      for (int u = 1; u < 5; ++u) hs += *lds<v2f>(L_HR + u * 32 + g * 8);
      hv[0] = fmaxf(rbf(hs[0]), 0.f); hv[1] = fmaxf(rbf(hs[1]), 0.f);
      float a[8] = {0.f, 0.f, 0.f, 0.f, 0.f, 0.f, 0.f, 0.f};
#pragma unroll
      for (int t = 0; t < 2; ++t) fma8(a, a2[t], hv[t]);
      red8(a, y0, y1);
    }
    v4u cx;
    _Pragma("unroll 1") for (u32 it = 0; it < SPIN_CAP; ++it) {
      CLOB();
      cx = ldc(rws, (l & 7) * 64 + g * 16, WS_CX + h * 4096 + j * 512);
      if (__all(max4u(cx) != SENT)) break;
      __builtin_amdgcn_s_sleep(SL4);
    }
    STAMP(12);
    float a[8] = {0.f, 0.f, 0.f, 0.f, 0.f, 0.f, 0.f, 0.f};
#pragma unroll
    for (int t = 0; t < 4; ++t) {
      float c = u2f(cx[t]);
      c += dpp<DPP_X1>(c); c += dpp<DPP_X2>(c); c += dpp<DPP_HMIR>(c);
      fma8(a, ow[t], rbf(c));
    }
    float s0, s1;
    red8(a, s0, s1);
    stc((v4u){canon(f2u(s0)), canon(f2u(y0)), canon(f2u(s1)), canon(f2u(y1))}, rws, l15 * 16384 + g * 16, WS_F + w * 16 * 16384 + bi * 64);
    if (l == 0) __hip_atomic_store(lds<u32>(L_CNT + 4), 100u, __ATOMIC_RELAXED, __HIP_MEMORY_SCOPE_WORKGROUP);
    STAMP(13);
  } else if (w < 7 && bi < 160) {
    const u32 rid = 2 * bi + (w - 5);
    const u32 xv = __builtin_amdgcn_raw_buffer_load_b32(rx, rid * 4, 0, 0);
    _Pragma("unroll 1") for (u32 it = 0; it < SPIN_CAP * 16; ++it) {
      if (__hip_atomic_load(lds<u32>(L_CNT + 4), __ATOMIC_RELAXED, __HIP_MEMORY_SCOPE_WORKGROUP) == 100u) break;
      __builtin_amdgcn_s_sleep(1);
    }
    __builtin_amdgcn_s_sleep(DL5);
    v4u f[4];
    _Pragma("unroll 1") for (u32 it = 0; it < SPIN_CAP; ++it) {
      CLOB();
#pragma unroll
      for (int t = 0; t < 4; ++t) f[t] = ldc(rws, l * 64 + t * 4096, WS_F + (rid >> 2) * 16384 + (rid & 3) * 16);
      if (__all(max3u(max3u(max4u(f[0]), max4u(f[1]), max4u(f[2])), max4u(f[3]), 0u) != SENT)) break;
      __builtin_amdgcn_s_sleep(SL5);
    }
    STAMP(12);
    float v[4];
#pragma unroll
    for (int e = 0; e < 4; ++e) v[e] = (u2f(f[0][e]) + u2f(f[1][e])) + (u2f(f[2][e]) + u2f(f[3][e]));
    float sv = tr32(v[0], v[2]), yv = tr32(v[1], v[3]);
    sv = sum16(sv); yv = sum16(yv);
    sv = rbf(tr16(sv, sv)); yv = rbf(tr16(yv, yv));
    const float gt = rbf(yv / (1.f + __builtin_amdgcn_exp2f(-1.4426950408889634f * yv)));
    const float xx = (l < 32) ? lo2f(xv) : hi2f(xv);
    const u32 ob = pk2(xx + rbf(sv * gt), 0.f);
    if ((l & 31) == 0) out[2 * rid + (l >> 5)] = (u16)ob;
    if (rid == 0 && l == 0) __builtin_amdgcn_raw_buffer_store_b32(tag, rws, 0, WS_EPOCH, 16);
    STAMP(13);
  }
  if (l == 0) {
    u32* sp = (u32*)(ws + WS_STAMP) + (bi * 8 + w) * NST;
    st[NST - 1] = (__builtin_amdgcn_s_getreg(63508) & 15) | (((u32)(__builtin_amdgcn_s_memtime() - clk0)) << 4);
    st[NST - 2] = (u32)__builtin_amdgcn_s_memrealtime();
#pragma unroll
    for (int k = 0; k < NST; ++k) sp[k] = st[k];
  }
}

#define NWV 2048

__global__ void report_k(const char* ws) {
  const u32* st = (const u32*)(ws + WS_STAMP);
  const int t = threadIdx.x;
  const u32 beg = (u32)__builtin_amdgcn_s_memrealtime();
  u32 t0 = 0xFFFFFFFFu;
  for (int i = t; i < NWV; i += 64) t0 = min(t0, st[i * NST]);
  for (int o = 32; o; o >>= 1) t0 = min(t0, (u32)__shfl_xor((int)t0, o));
  u32 mn = 0xFFFFFFFFu, mx = 0, sm = 0, ct = 0;
  for (int i = t; i < NWV; i += 64) { u32 v = st[i * NST + (0)]; if (v) { v -= t0; mn = min(mn, v); mx = max(mx, v); sm += v; ct++; } }
  for (int o = 32; o; o >>= 1) { mn = min(mn, (u32)__shfl_xor((int)mn, o)); mx = max(mx, (u32)__shfl_xor((int)mx, o)); sm += __shfl_xor((int)sm, o); ct += __shfl_xor((int)ct, o); }
  const u32 av = ct ? sm / ct : 0;
  u32 q = (0);
  if (q > 5000) q = 5000;
  const u32 dur = 10000 + 20 * q;
  while ((u32)__builtin_amdgcn_s_memrealtime() - beg < dur) __builtin_amdgcn_s_sleep(1);
}

extern "C" void kernel_launch(void* const* d_in, const int* in_sizes, int n_in, void* d_out, int out_size, void* d_ws, size_t ws_size,
                              hipStream_t stream) {
  (void)in_sizes; (void)n_in; (void)out_size; (void)ws_size;
  (void)hipFuncSetAttribute((const void*)mla_ffn_kernel, hipFuncAttributeMaxDynamicSharedMemorySize, L_TOTAL);
  mla_ffn_kernel<<<256, 512, L_TOTAL, stream>>>((const u16*)d_in[0], (const u16*)d_in[1], (const u16*)d_in[2], (const u16*)d_in[3],
                                                (const u16*)d_in[4], (const u16*)d_in[5], (const u16*)d_in[7], (const u16*)d_in[8],
                                                (u16*)d_out, (char*)d_ws);
  report_k<<<1, 64, 0, stream>>>((const char*)d_ws);
}
```

```cpp
#include <hip/hip_runtime.h>
#include <hip/hip_bf16.h>
#include <stdint.h>

typedef unsigned short u16;
typedef unsigned int u32;
typedef short v4s __attribute__((ext_vector_type(4)));
typedef __bf16 v8bf __attribute__((ext_vector_type(8)));
typedef __bf16 v2bf __attribute__((ext_vector_type(2)));
typedef float v4f __attribute__((ext_vector_type(4)));
typedef float v2f __attribute__((ext_vector_type(2)));
typedef u32 v4u __attribute__((ext_vector_type(4)));
typedef u32 v2u __attribute__((ext_vector_type(2)));
#define LAS __attribute__((address_space(3)))
typedef __amdgpu_buffer_rsrc_t rsrc_t;

#define KNOB(n, v) constexpr int n = v
#define W_AUX 0
#define KV_NT ""
#define W1A 8
#define W1B 16
#define W0A 16
#define H0ROWS 24
#define AROWS 32
#define START0 1
#define HINT_AT 1
#define GPOS 3
#define LATE_EARLY 0
#define ML_LATE 0
#define DL1 8
#define SL1 1
#define DLG 40
#define SLG 2
#define DL2 0
#define SL2 8
#define SL3 2
#define DL3 0
#define DL4 6
#define SL4 1
#define SL5 2
#define DL5 10
#define STR2(x) #x
#define STR(x) STR2(x)

constexpr u32 WS_EPOCH = 0;
constexpr u32 WS_HINT = 4096;
constexpr u32 WS_Q1 = WS_HINT + 8192;
constexpr u32 WS_QA = WS_Q1 + 131072;
constexpr u32 WS_ML = WS_QA + 65536;
constexpr u32 WS_CX = WS_ML + 65536;
constexpr u32 WS_F = WS_CX + 262144;
constexpr u32 WS_OP = WS_F + 80 * 8192;
constexpr u32 WS_END = WS_OP + 256 * 16384;
constexpr u32 WS_STAMP = WS_END;
static_assert(WS_F == WS_CX + 262144 && (262144 + 80 * 8192) == 128 * 7168, "clear split");

constexpr u32 RS = 1040;
constexpr u32 L_KV = 0;
constexpr u32 L_QA = 128 * RS;
constexpr u32 L_P = L_QA + 17408;
constexpr u32 L_MX = L_P + 4096;
constexpr u32 L_LS = L_MX + 512;
constexpr u32 L_QR = L_LS + 512;
constexpr u32 L_OA = L_QR + 2560;
constexpr u32 L_ML = L_OA + 16 * 144;
constexpr u32 L_CNT = L_ML + 64;
constexpr u32 L_TOTAL = L_CNT + 64;
constexpr u32 UVS = 272;
static_assert(64 * UVS <= 17408, "uv image");
static_assert(L_TOTAL <= 163840, "lds");

constexpr u32 SENT = 0xFFFFFFFFu;
constexpr u32 SPIN_CAP = 1u << 18;

__device__ __forceinline__ float u2f(u32 u) { return __uint_as_float(u); }
__device__ __forceinline__ u32 f2u(float f) { return __float_as_uint(f); }
__device__ __forceinline__ float lo2f(u32 u) { return __uint_as_float(u << 16); }
__device__ __forceinline__ float hi2f(u32 u) { return __uint_as_float(u & 0xFFFF0000u); }
__device__ __forceinline__ u32 pk2(float a, float b) { v2f f = {a, b}; v2bf c = __builtin_convertvector(f, v2bf); return *(u32*)&c; }
__device__ __forceinline__ float rbf(float a) { return lo2f(pk2(a, a)); }
__device__ __forceinline__ u32 canon(u32 u) { return u < 0xFFFFFFFEu ? u : 0xFFFFFFFEu; }
__device__ __forceinline__ u32 max3u(u32 a, u32 b, u32 c) { u32 m = a > b ? a : b; return m > c ? m : c; }
__device__ __forceinline__ u32 max4u(v4u v) { return max3u(max3u(v[0], v[1], v[2]), v[3], v[3]); }
template <int CTRL> __device__ __forceinline__ float dpp(float v) { return u2f(__builtin_amdgcn_mov_dpp(f2u(v), CTRL, 0xF, 0xF, true)); }
constexpr int DPP_X1 = 0xB1, DPP_X2 = 0x4E, DPP_ROR4 = 0x124, DPP_ROR8 = 0x128, DPP_HMIR = 0x141;
__device__ __forceinline__ float sum16(float v) { v += dpp<DPP_X1>(v); v += dpp<DPP_X2>(v); v += dpp<DPP_ROR4>(v); v += dpp<DPP_ROR8>(v); return v; }
__device__ __forceinline__ float max16(float v) { v = fmaxf(v, dpp<DPP_X1>(v)); v = fmaxf(v, dpp<DPP_X2>(v)); v = fmaxf(v, dpp<DPP_ROR4>(v)); v = fmaxf(v, dpp<DPP_ROR8>(v)); return v; }
__device__ __forceinline__ float tr16(float a, float b) { auto r = __builtin_amdgcn_permlane16_swap(f2u(a), f2u(b), false, false); u32 x = r[0], y = r[1]; return u2f(x) + u2f(y); }
__device__ __forceinline__ float tr32(float a, float b) { auto r = __builtin_amdgcn_permlane32_swap(f2u(a), f2u(b), false, false); u32 x = r[0], y = r[1]; return u2f(x) + u2f(y); }
__device__ __forceinline__ float mx16(float a) { auto r = __builtin_amdgcn_permlane16_swap(f2u(a), f2u(a), false, false); u32 x = r[0], y = r[1]; return fmaxf(u2f(x), u2f(y)); }
__device__ __forceinline__ float mx32(float a) { auto r = __builtin_amdgcn_permlane32_swap(f2u(a), f2u(a), false, false); u32 x = r[0], y = r[1]; return fmaxf(u2f(x), u2f(y)); }
__device__ __forceinline__ float sum64(float v) { v = sum16(v); v = tr16(v, v); return tr32(v, v); }
__device__ __forceinline__ float max64(float v) { v = max16(v); v = mx16(v); return mx32(v); }
__device__ __forceinline__ float dot2(u32 a, u32 b, float c) { return __builtin_amdgcn_fdot2_f32_bf16(*(v2bf*)&a, *(v2bf*)&b, c, false); }
__device__ __forceinline__ v4f mfma(v4u a, v4u b, v4f c) { return __builtin_amdgcn_mfma_f32_16x16x32_bf16(*(v8bf*)&a, *(v8bf*)&b, c, 0, 0, 0); }
__device__ __forceinline__ rsrc_t mk(const void* p, u32 n) { return __builtin_amdgcn_make_buffer_rsrc((void*)p, 0, n, 0x00020000); }
__device__ __forceinline__ v4u ldw(rsrc_t r, u32 v, u32 s) { return __builtin_amdgcn_raw_buffer_load_b128(r, v, s, W_AUX); }
__device__ __forceinline__ v4u ldc(rsrc_t r, u32 v, u32 s) { return __builtin_amdgcn_raw_buffer_load_b128(r, v, s, 16); }
__device__ __forceinline__ v2u ldc2(rsrc_t r, u32 v, u32 s) { return __builtin_amdgcn_raw_buffer_load_b64(r, v, s, 16); }
__device__ __forceinline__ void stc(v4u d, rsrc_t r, u32 v, u32 s) {
  __builtin_amdgcn_raw_buffer_store_b128(d, r, v, s, 16);
  asm volatile("s_nop 1" :: "v"(d));
}
__device__ __forceinline__ void stc2(v2u d, rsrc_t r, u32 v, u32 s) { __builtin_amdgcn_raw_buffer_store_b64(d, r, v, s, 16); }
__device__ __forceinline__ u16 ldx(rsrc_t r, u32 elem) { return __builtin_amdgcn_raw_buffer_load_b16(r, elem * 2, 0, 0); }
#define BAR() asm volatile("s_waitcnt lgkmcnt(0)\n\ts_barrier" ::: "memory")
#define SCHED() __builtin_amdgcn_sched_barrier(0)
#define CLOB() asm volatile("" ::: "memory")
#define CVM0() __builtin_amdgcn_s_waitcnt(0x0F70)
#define VM0() asm volatile("s_waitcnt vmcnt(0)" ::: "memory")
template <typename T> __device__ __forceinline__ LAS T* lds(u32 off) { return (LAS T*)(uintptr_t)off; }
__device__ __forceinline__ void fma8(float* a, v4u w, float x) {
#pragma unroll
  for (int e = 0; e < 4; ++e) { a[2 * e] = fmaf(lo2f(w[e]), x, a[2 * e]); a[2 * e + 1] = fmaf(hi2f(w[e]), x, a[2 * e + 1]); }
}
__device__ __forceinline__ void red8(const float* a, float& o0, float& o1) {
  float t0 = tr32(a[0], a[4]), t1 = tr32(a[1], a[5]), t2 = tr32(a[2], a[6]), t3 = tr32(a[3], a[7]);
  o0 = tr16(t0, t2); o1 = tr16(t1, t3);
}

__device__ __forceinline__ u32 ldsld(u32 off) { return __hip_atomic_load(lds<u32>(off), __ATOMIC_RELAXED, __HIP_MEMORY_SCOPE_WORKGROUP); }
__device__ __forceinline__ void ldsst(u32 off, u32 v) { __hip_atomic_store(lds<u32>(off), v, __ATOMIC_RELAXED, __HIP_MEMORY_SCOPE_WORKGROUP); }
__device__ __forceinline__ void wait_ge(u32 off, u32 v) {
  _Pragma("unroll 1") for (u32 it = 0; it < SPIN_CAP * 16; ++it) {
    if (ldsld(off) >= v) break;
    __builtin_amdgcn_s_sleep(1);
  }
  CLOB();
}
__device__ __forceinline__ void wait_kv(u32 v) {
  _Pragma("unroll 1") for (u32 it = 0; it < SPIN_CAP * 16; ++it) {
    const u32 a = ldsld(L_CNT + 8), b = ldsld(L_CNT + 12);
    if ((a < b ? a : b) >= v) break;
    __builtin_amdgcn_s_sleep(1);
  }
  CLOB();
}
template <int W> __device__ __forceinline__ void dma_row(const char* src, u32 voff, u32 dst) {
  u32 keep;
  asm volatile("s_waitcnt vmcnt(%4)\n\ts_mov_b32 %0, m0\n\ts_mov_b32 m0, %3\n\ts_nop 0\n\tglobal_load_lds_dwordx4 %1, %2" KV_NT "\n\ts_mov_b32 m0, %0"
               : "=&s"(keep) : "v"(voff), "s"(src), "s"(dst), "n"(W - 1) : "memory");
}

#define STAMP(k) do {} while (0)

__global__ void __launch_bounds__(512, 2)
mla_ffn_kernel(const u16* x, const u16* ckv, const u16* wq, const u16* wuk,
               const u16* wuv, const u16* wo, const u16* w1, const u16* w2,
               u16* out, char* ws) {
  extern __shared__ __attribute__((aligned(16))) char smem_[];
  STAMP(0);
  asm volatile("" :: "s"(x), "s"(ckv), "s"(wq), "s"(wuk), "s"(wuv), "s"(wo), "s"(w1), "s"(w2), "s"(out), "s"(ws));
  u32 tag;
  asm volatile("s_load_dword %0, %1, 0x0" : "=s"(tag) : "s"(ws) : "memory");
  const u32 bi = blockIdx.x, xc = bi & 7, r = bi >> 3;
  const u32 type = r & 1, pi = r >> 1;
  const u32 chunk = xc * 16 + pi;
  const u32 h = (1 - type) * 16 + xc * 2 + (pi >> 3), j = pi & 7;
  const u32 hq = xc * 4 + (pi >> 2), p = pi & 3;
  const u32 w = __builtin_amdgcn_readfirstlane(threadIdx.x >> 6), l = threadIdx.x & 63;
  const u32 l15 = l & 15, g = l >> 4;
  const rsrc_t rws = mk(ws, WS_END);
  const rsrc_t rx = mk(x, 1280);
  float xn1[2] = {0.f, 0.f};
  if (threadIdx.x < 8) *lds<u32>(L_CNT + threadIdx.x * 4) = 0u;

  if (w < 5) {
    const v4u xa = __builtin_amdgcn_raw_buffer_load_b128(rx, l * 16, 0, 0);
    const u32 xb = __builtin_amdgcn_raw_buffer_load_b32(rx, l * 4, 1024, 0);
    const u32 x1 = __builtin_amdgcn_raw_buffer_load_b32(rx, (l >> 3) * 4, (80 * j + 16 * w) * 2, 0);
    const u32 m0 = 160 * p + 32 * w;
    const v4u xr = __builtin_amdgcn_raw_buffer_load_b128(rx, g * 16, m0 * 2, 0);
    const rsrc_t rq = mk(wq, type == 0 ? 640 * 4096 * 2 : 0);
    const rsrc_t rk = mk(wuk, type == 0 && w < 4 ? 512 * 4096 * 2 : 0);
    v4u qv[8], kv[8];
#pragma unroll
    for (int t = 0; t < 8; ++t) qv[t] = ldw(rq, g * 65536 + l15 * 16, (m0 + t) * 8192 + hq * 256);
    SCHED();
    BAR();
    SCHED();
#pragma unroll
    for (int t = 0; t < 8; ++t) kv[t] = ldw(rk, g * 65536 + l15 * 16, (128 * p + 32 * w + t) * 8192 + hq * 256);
    SCHED();
    asm volatile("" : "+s"(tag));
    tag += 1u;
    float s = lo2f(xb) + hi2f(xb);
#pragma unroll
    for (int k = 0; k < 4; ++k) s += lo2f(xa[k]) + hi2f(xa[k]);
    const float mean = sum64(s) * (1.f / 640.f);
    float v;
    { float a = lo2f(xb) - mean, b = hi2f(xb) - mean; v = a * a; v = fmaf(b, b, v); }
#pragma unroll
    for (int k = 0; k < 4; ++k) { float a = lo2f(xa[k]) - mean, b = hi2f(xa[k]) - mean; v = fmaf(a, a, v); v = fmaf(b, b, v); }
    v = rbf(sum64(v) * (1.f / 640.f));
    const float mean_b = rbf(mean);
    const float sd_b = rbf(sqrtf(rbf(v + 1e-5f)));
    xn1[0] = rbf(rbf(lo2f(x1) - mean_b) / sd_b);
    xn1[1] = rbf(rbf(hi2f(x1) - mean_b) / sd_b);
    float xq[8];
#pragma unroll
    for (int t = 0; t < 8; ++t) { const u32 xw = xr[t >> 1]; xq[t] = rbf(rbf(((t & 1) ? hi2f(xw) : lo2f(xw)) - mean_b) / sd_b); }
    asm volatile("" : "+v"(xn1[0]), "+v"(xn1[1]), "+v"(xq[0]), "+v"(xq[1]), "+v"(xq[2]), "+v"(xq[3]), "+v"(xq[4]), "+v"(xq[5]), "+v"(xq[6]), "+v"(xq[7]));
    STAMP(1);
    v4u pq[4];
    if (type == 0) {
      float a[8] = {0.f, 0.f, 0.f, 0.f, 0.f, 0.f, 0.f, 0.f};
#pragma unroll
      for (int t = 0; t < 8; ++t) fma8(a, qv[t], xq[t]);
      float o0, o1;
      red8(a, o0, o1);
      *lds<v2f>(L_QR + w * 512 + (l15 * 8 + g * 2) * 4) = (v2f){o0, o1};
      asm volatile("s_waitcnt lgkmcnt(0)" ::: "memory");
      u32 arrived = 0;
      if (l == 0) arrived = __hip_atomic_fetch_add(lds<u32>(L_CNT), 1u, __ATOMIC_RELAXED, __HIP_MEMORY_SCOPE_WORKGROUP);
      arrived = __builtin_amdgcn_readfirstlane(arrived);
      if (arrived == 5) {
        CLOB();
        v2f sacc = *lds<v2f>(L_QR + l * 8);
#pragma unroll
        for (int u = 1; u < 5; ++u) sacc += *lds<v2f>(L_QR + u * 512 + l * 8);
        stc((v4u){f2u(sacc[0]), tag, f2u(sacc[1]), tag}, rws, l * 16, WS_Q1 + hq * 4096 + p * 1024);
        if (HINT_AT == 0 && l < 8) stc2((v2u){tag, tag}, rws, l * 1024, WS_HINT + (hq * 4 + p) * 8);
        ldsst(L_CNT + 4, 1u);
      }
      STAMP(2);
      if (w < 4) {
        wait_ge(L_CNT + 4, 1u);
        __builtin_amdgcn_s_sleep(DL1);
        _Pragma("unroll 1") for (u32 it = 0; it < SPIN_CAP; ++it) {
          CLOB();
          u32 bad = 0;
#pragma unroll
          for (int k = 0; k < 4; ++k) pq[k] = ldc(rws, (l15 * 4 + g) * 16 + k * 1024, WS_Q1 + hq * 4096);
          if (START0 == 2) ldsst(L_CNT + 4, 2u);
#pragma unroll
          for (int k = 0; k < 4; ++k) bad |= (pq[k][1] ^ tag) | (pq[k][3] ^ tag);
          if (__all(bad == 0)) break;
          __builtin_amdgcn_s_sleep(SL1);
        }
      }
    }
    CVM0();
    if (type == 0) {
      if (w < 4) {
        if (HINT_AT == 1 && w == 0 && l < 8) stc2((v2u){tag, tag}, rws, l * 1024, WS_HINT + (hq * 4 + p) * 8);
        STAMP(3);
        const u32 qp = pk2((u2f(pq[0][0]) + u2f(pq[1][0])) + (u2f(pq[2][0]) + u2f(pq[3][0])), (u2f(pq[0][2]) + u2f(pq[1][2])) + (u2f(pq[2][2]) + u2f(pq[3][2])));
        auto s16 = __builtin_amdgcn_permlane16_swap(qp, qp, false, false);
        const u32 e02 = s16[0], e13 = s16[1];
        auto sa = __builtin_amdgcn_permlane32_swap(e02, e02, false, false);
        auto sb = __builtin_amdgcn_permlane32_swap(e13, e13, false, false);
        const u32 q0 = sa[0], q2 = sa[1], q1 = sb[0], q3 = sb[1];
        float d[8];
#pragma unroll
        for (int t = 0; t < 8; ++t) {
          float acc = dot2(kv[t][0], q0, 0.f);
          acc = dot2(kv[t][1], q1, acc); acc = dot2(kv[t][2], q2, acc); acc = dot2(kv[t][3], q3, acc);
          d[t] = acc;
        }
        float e[4];
#pragma unroll
        for (int t = 0; t < 4; ++t) {
          const float keep = (l & 1) ? d[t + 4] : d[t], give = (l & 1) ? d[t] : d[t + 4];
          float sum = keep + dpp<DPP_X1>(give);
          sum += dpp<DPP_X2>(sum); sum += dpp<DPP_ROR4>(sum); sum += dpp<DPP_ROR8>(sum);
          e[t] = sum * (0.08838834764831845f * 1.4426950408889634f);
        }
        if (l15 < 2) stc((v4u){pk2(e[0], e[1]), tag, pk2(e[2], e[3]), tag}, rws, g * 32 + l15 * 16, WS_QA + hq * 2048 + p * 512 + w * 128);
        STAMP(4);
      }
    }
  } else if (w == 5) {
    const v4u sv = {SENT, SENT, SENT, SENT};
    BAR();
    asm volatile("" : "+s"(tag));
    tag += 1u;
    if (type == 0) {
#pragma unroll
      for (int k = 0; k < 7; ++k) stc(sv, rws, l * 16 + k * 1024, WS_CX + chunk * 7168);
      VM0();
      u32 arrived = 0;
      if (l == 0) arrived = __hip_atomic_fetch_add(lds<u32>(L_CNT), 1u, __ATOMIC_RELAXED, __HIP_MEMORY_SCOPE_WORKGROUP);
      arrived = __builtin_amdgcn_readfirstlane(arrived);
      if (arrived == 5) {
        CLOB();
        v2f sacc = *lds<v2f>(L_QR + l * 8);
#pragma unroll
        for (int u = 1; u < 5; ++u) sacc += *lds<v2f>(L_QR + u * 512 + l * 8);
        stc((v4u){f2u(sacc[0]), tag, f2u(sacc[1]), tag}, rws, l * 16, WS_Q1 + hq * 4096 + p * 1024);
        if (HINT_AT == 0 && l < 8) stc2((v2u){tag, tag}, rws, l * 1024, WS_HINT + (hq * 4 + p) * 8);
        ldsst(L_CNT + 4, 1u);
      }
    }
    STAMP(1);
#pragma unroll 1
    for (u32 k = 0; k < 16; ++k) stc(sv, rws, (l >> 3) * 16384 + (l & 7) * 16, WS_OP + (type * 16 + k) * 131072 + chunk * 128);
    STAMP(2);
    __builtin_amdgcn_s_sleep(DLG);
    _Pragma("unroll 1") for (u32 it = 0; it < SPIN_CAP; ++it) {
      CLOB();
      const v2u hv = ldc2(rws, l * 8, WS_HINT + xc * 1024 + type * 512);
      if (__all(hv[0] == tag)) break;
      __builtin_amdgcn_s_sleep(SLG);
    }
    __builtin_amdgcn_s_sleep(DL2);
    ldsst(L_CNT + 16, 1u);
    STAMP(3);
  } else {
    BAR();
    asm volatile("" : "+s"(tag));
    tag += 1u;
    const char* src = (const char*)ckv + (size_t)chunk * 131072;
    const u32 lw = w - 6;
    if (type == 0) {
      if (START0) wait_ge(L_CNT + 4, START0);
      STAMP(1);
#pragma unroll 1
      for (u32 s = 0; s < 4; ++s) {
#pragma unroll 1
        for (u32 n = 0; n < 16; ++n) {
          const u32 slot = 32 * s + 2 * n + lw;
          const u32 row = slot < H0ROWS ? slot : slot < H0ROWS + AROWS ? slot + (128 - AROWS - H0ROWS) : slot - AROWS;
          dma_row<W0A>(src + row * 1024, l * 16, slot * RS);
        }
        asm volatile("s_waitcnt vmcnt(16)" ::: "memory");
        ldsst(L_CNT + 8 + 4 * lw, s);
      }
    } else {
      STAMP(1);
#pragma unroll 1
      for (u32 n = 0; n < 32; ++n) {
        const u32 slot = 2 * n + lw;
        dma_row<W1A>(src + slot * 1024, l * 16, slot * RS);
        if (n == 15 + W1A) ldsst(L_CNT + 8 + 4 * lw, 1u);
      }
      STAMP(2);
#pragma unroll 1
      for (u32 s = 2; s < 4; ++s) {
#pragma unroll 1
        for (u32 n = 0; n < 16; ++n) {
          const u32 slot = 32 * s + 2 * n + lw;
          dma_row<W1B>(src + slot * 1024, l * 16, slot * RS);
        }
        asm volatile("s_waitcnt vmcnt(16)" ::: "memory");
        ldsst(L_CNT + 8 + 4 * lw, s);
      }
    }
    STAMP(3);
  }

  const bool ffn = (h < 27) && (w < 5);
  v4u af[16];
  v2u tv[2][2][4][2];
  v4u pa[4];
  v4u uv[2], a1[2], ow[4];
  auto late = [&]() {
    const rsrc_t ru = mk(wuv, 512 * 4096 * 2);
#pragma unroll
    for (int t = 0; t < 2; ++t) {
      const u32 k = 8 * w + 4 * t + g;
      const u32 G = w & 3, a = w >> 2, sg = ((G & 1) << 1) | (G >> 1);
      const u32 c = 8 * ((j >> 2) * 32 + (j & 3) * 2 + a + 8 * sg) + ((k & 7) ^ ((G & 1) << 2));
      uv[t] = ldw(ru, c * 8192 + l15 * 16, h * 256);
    }
    const rsrc_t r1 = mk(w1, ffn ? 640 * 1728 * 2 : 0);
#pragma unroll
    for (int t = 0; t < 2; ++t) a1[t] = ldw(r1, (l >> 3) * 6912 + (l & 7) * 16, (80 * j + 16 * w + t) * 3456 + h * 128);
    const rsrc_t ro = mk(wo, w < 5 ? 4096 * 640 * 2 : 0);
#pragma unroll
    for (int t = 0; t < 4; ++t) ow[t] = ldw(ro, (4 * g + t) * 1280 + l15 * 16, (h * 128 + j * 16) * 1280 + w * 256);
  };
  {
    const u32 q = l15 >> 2, pp = l & 3, sg = ((pp & 1) << 1) | (pp >> 1);
    const u32 base = L_KV + (4 * g + q) * RS + ((w >> 2) * 32 + (w & 3) * 2 + 8 * sg) * 16;
    const u32 ab = L_KV + (16 * w + l15) * RS + g * 256;
#pragma unroll
    for (int s = 0; s < 4; ++s) {
      if (s == GPOS) {
        wait_ge(L_CNT + 16, 1u);
        STAMP(15);
#pragma unroll
        for (int k = 0; k < 4; ++k) pa[k] = ldc(rws, l * 16 + k * 1024, WS_QA + type * 32768 + w * 4096);
        if (LATE_EARLY) late();
        SCHED();
      }
      if (s == 3 && w >= 6) {
        asm volatile("s_waitcnt vmcnt(%0)" :: "n"(GPOS <= 3 ? (LATE_EARLY ? 12 : 4) : 0) : "memory");
        ldsst(L_CNT + 8 + 4 * (w - 6), 4u);
      }
      wait_kv(s + 1);
      if (s == 3) STAMP(20);
      if ((w >> 1) == s) {
#pragma unroll
        for (int k = 0; k < 16; ++k) af[k] = *lds<v4u>(ab + k * 16);
      }
#pragma unroll
      for (int a = 0; a < 2; ++a)
#pragma unroll
        for (int T = 0; T < 2; ++T)
#pragma unroll
          for (int t = 0; t < 2; ++t) {
            v4s rv = __builtin_amdgcn_ds_read_tr16_b64_v4i16(lds<v4s>(base + 8 * ((pp & 1) ^ T) + (32 * s + 16 * t) * RS + a * 16));
            tv[a][T][s][t] = *(v2u*)&rv;
          }
      SCHED();
    }
    if (GPOS == 4) {
      wait_ge(L_CNT + 16, 1u);
      STAMP(15);
#pragma unroll
      for (int k = 0; k < 4; ++k) pa[k] = ldc(rws, l * 16 + k * 1024, WS_QA + type * 32768 + w * 4096);
      if (LATE_EARLY) late();
      SCHED();
    }
    _Pragma("unroll 1") for (u32 it = 0; it < SPIN_CAP; ++it) {
      u32 bad = 0;
#pragma unroll
      for (int k = 0; k < 4; ++k) bad |= (pa[k][1] ^ tag) | (pa[k][3] ^ tag);
      if (__all(bad == 0)) break;
      __builtin_amdgcn_s_sleep(SL2);
      CLOB();
#pragma unroll
      for (int k = 0; k < 4; ++k) pa[k] = ldc(rws, l * 16 + k * 1024, WS_QA + type * 32768 + w * 4096);
    }
#pragma unroll
    for (int k = 0; k < 4; ++k) *lds<v2u>(L_QA + (2 * w + (k >> 1)) * RS + (k & 1) * 512 + l * 8) = (v2u){pa[k][0], pa[k][2]};
  }
  STAMP(5);
  if (!LATE_EARLY) late();
  SCHED();
  BAR();
  STAMP(6);

  v4f sacc = {0.f, 0.f, 0.f, 0.f};
  {
    const u32 bb = L_QA + l15 * RS + g * 256;
    v4u bq[16];
#pragma unroll
    for (int s = 0; s < 12; ++s) bq[s] = *lds<v4u>(bb + s * 16);
    SCHED();
#pragma unroll
    for (int s = 0; s < 4; ++s) { sacc = mfma(af[s], bq[s], sacc); bq[12 + s] = *lds<v4u>(bb + (12 + s) * 16); SCHED(); }
#pragma unroll
    for (int s = 4; s < 16; ++s) sacc = mfma(af[s], bq[s], sacc);
  }
  float mx = fmaxf(fmaxf(sacc[0], sacc[1]), fmaxf(sacc[2], sacc[3]));
  mx = mx32(mx16(mx));
  if (g == 0) *lds<float>(L_MX + l15 * 32 + w * 4) = mx;
  BAR();
  STAMP(17);
  {
    v4f m0 = *lds<v4f>(L_MX + l15 * 32), m1 = *lds<v4f>(L_MX + l15 * 32 + 16);
    mx = fmaxf(fmaxf(fmaxf(m0[0], m0[1]), fmaxf(m0[2], m0[3])), fmaxf(fmaxf(m1[0], m1[1]), fmaxf(m1[2], m1[3])));
  }
  {
    const float e0 = __builtin_amdgcn_exp2f(sacc[0] - mx), e1 = __builtin_amdgcn_exp2f(sacc[1] - mx);
    const float e2 = __builtin_amdgcn_exp2f(sacc[2] - mx), e3 = __builtin_amdgcn_exp2f(sacc[3] - mx);
    *lds<v2u>(L_P + (w >> 1) * 1024 + l * 16 + (w & 1) * 8) = (v2u){pk2(e0, e1), pk2(e2, e3)};
    float ls = (e0 + e1) + (e2 + e3);
    ls = tr16(ls, ls); ls = tr32(ls, ls);
    if (g == 0) *lds<float>(L_LS + l15 * 32 + w * 4) = ls;
  }
  BAR();
  STAMP(7);
  {
    v4u pf[4];
#pragma unroll
    for (int s = 0; s < 4; ++s) pf[s] = *lds<v4u>(L_P + s * 1024 + l * 16);
    auto pub_ml = [&]() {
      if (w == 7) {
        v4f a = *lds<v4f>(L_LS + l15 * 32), b = *lds<v4f>(L_LS + l15 * 32 + 16);
        const float lt = ((a[0] + a[1]) + (a[2] + a[3])) + ((b[0] + b[1]) + (b[2] + b[3]));
        if (g == 0) stc((v4u){f2u(mx), tag, f2u(lt), tag}, rws, l15 * 2048, WS_ML + type * 32768 + chunk * 16);
      }
    };
    if (!ML_LATE) pub_ml();
    v4u ov[2];
#pragma unroll
    for (int a = 0; a < 2; ++a) {
      v4f o[2];
#pragma unroll
      for (int T = 0; T < 2; ++T) {
        o[T] = (v4f){0.f, 0.f, 0.f, 0.f};
#pragma unroll
        for (int s = 0; s < 4; ++s) {
          v4u av = {tv[a][T][s][0][0], tv[a][T][s][0][1], tv[a][T][s][1][0], tv[a][T][s][1][1]};
          o[T] = mfma(av, pf[s], o[T]);
        }
      }
      ov[a] = (v4u){canon(pk2(o[0][0], o[0][1])), canon(pk2(o[0][2], o[0][3])), canon(pk2(o[1][0], o[1][1])), canon(pk2(o[1][2], o[1][3]))};
    }
    v4u s0, s1;
#pragma unroll
    for (int e = 0; e < 4; ++e) {
      const u32 a0 = ov[0][e], b0 = ov[1][e];
      s0[e] = __builtin_amdgcn_update_dpp(a0, b0, DPP_ROR8, 0xF, 0xC, false);
      s1[e] = __builtin_amdgcn_update_dpp(b0, a0, DPP_ROR8, 0xF, 0x3, false);
    }
    const u32 vo = (l15 & 7) * 131072 + g * 16 + (l15 >> 3) * 64;
    stc(s0, rws, vo, WS_OP + (type * 128 + w) * 16384 + chunk * 128);
    stc(s1, rws, vo, WS_OP + (type * 128 + 64 + w) * 16384 + chunk * 128);
    if (ML_LATE) pub_ml();
  }
  STAMP(8);
#pragma unroll
  for (int t = 0; t < 2; ++t) *lds<v4u>(L_QA + (8 * w + 4 * t + g) * UVS + l15 * 16) = uv[t];
  if (w < 5) {
    float a[8] = {0.f, 0.f, 0.f, 0.f, 0.f, 0.f, 0.f, 0.f};
#pragma unroll
    for (int t = 0; t < 2; ++t) fma8(a, a1[t], xn1[t]);
    float o0, o1;
    red8(a, o0, o1);
    o0 += dpp<DPP_ROR8>(o0); o1 += dpp<DPP_ROR8>(o1);
    if (l15 < 8) *lds<v2f>(L_QR + w * 256 + (l15 * 8 + g * 2) * 4) = (v2f){o0, o1};
  }
  STAMP(9);
  v4u a2[2];
  {
    v4u op[2], ml[2];
    _Pragma("unroll 1") for (u32 it = 0; it < SPIN_CAP; ++it) {
      CLOB();
#pragma unroll
      for (int t = 0; t < 2; ++t) ml[t] = ldc(rws, (l >> 3) * 16 + t * 128, WS_ML + h * 2048 + w * 256);
      if (__all(((ml[0][1] ^ tag) | (ml[0][3] ^ tag) | (ml[1][1] ^ tag) | (ml[1][3] ^ tag)) == 0)) break;
      __builtin_amdgcn_s_sleep(SL3);
    }
    STAMP(16);
    __builtin_amdgcn_s_sleep(DL3);
    CLOB();
#pragma unroll
    for (int t = 0; t < 2; ++t) op[t] = ldc(rws, l * 16 + t * 1024, WS_OP + (h * 8 + j) * 16384 + w * 2048);
    const rsrc_t r2 = mk(w2, ffn ? 1728 * 640 * 2 : 0);
#pragma unroll
    for (int t = 0; t < 2; ++t) a2[t] = ldw(r2, (2 * g + t) * 1280 + l15 * 16, (64 * h + 8 * j) * 1280 + w * 256);
    SCHED();
    float M = fmaxf(u2f(ml[0][0]), u2f(ml[1][0]));
    M = fmaxf(M, dpp<DPP_ROR8>(M)); M = mx32(mx16(M));
    const float c0 = __builtin_amdgcn_exp2f(u2f(ml[0][0]) - M), c1 = __builtin_amdgcn_exp2f(u2f(ml[1][0]) - M);
    float L = c0 * u2f(ml[0][2]) + c1 * u2f(ml[1][2]);
    L += dpp<DPP_ROR8>(L); L = tr16(L, L); L = tr32(L, L);
    if (l == 0) *lds<v2f>(L_ML + w * 8) = (v2f){M, L};
    _Pragma("unroll 1") for (u32 it = 0; it < SPIN_CAP; ++it) {
      if (__all(max3u(max4u(op[0]), max4u(op[1]), 0u) != SENT)) break;
      __builtin_amdgcn_s_sleep(SL3);
      CLOB();
#pragma unroll
      for (int t = 0; t < 2; ++t) op[t] = ldc(rws, l * 16 + t * 1024, WS_OP + (h * 8 + j) * 16384 + w * 2048);
    }
    STAMP(10);
    float a[8] = {0.f, 0.f, 0.f, 0.f, 0.f, 0.f, 0.f, 0.f};
    fma8(a, op[0], c0); fma8(a, op[1], c1);
    float o0, o1;
    red8(a, o0, o1);
    o0 += dpp<DPP_ROR8>(o0); o1 += dpp<DPP_ROR8>(o1);
    const u32 hi = pk2(o0, o1), lo = pk2(o0 - lo2f(hi), o1 - hi2f(hi));
    *lds<u32>(L_OA + (2 * w + ((l >> 3) & 1)) * 144 + ((l & 7) * 8 + g * 2) * 2) = (l & 8) ? lo : hi;
  }
  BAR();
  STAMP(19);
  {
    v4f c = {0.f, 0.f, 0.f, 0.f};
#pragma unroll
    for (int s = 0; s < 2; ++s) {
      const u32 ta = L_QA + (32 * s + 8 * g + (l15 >> 2)) * UVS + w * 32 + (l & 3) * 8;
      v4s r0 = __builtin_amdgcn_ds_read_tr16_b64_v4i16(lds<v4s>(ta));
      v4s r1 = __builtin_amdgcn_ds_read_tr16_b64_v4i16(lds<v4s>(ta + 4 * UVS));
      v2u b0 = *(v2u*)&r0, b1 = *(v2u*)&r1;
      c = mfma(*lds<v4u>(L_OA + l15 * 144 + s * 64 + g * 16), (v4u){b0[0], b0[1], b1[0], b1[1]}, c);
    }
    const v2f mi = *lds<v2f>(L_ML + (l & 7) * 8);
    const v4f mg = *lds<v4f>(L_ML + g * 16);
    v4f hs = *lds<v4f>(L_QR + w * 32 + (l & 1) * 16);
#pragma unroll
    for (int u = 1; u < 5; ++u) hs += *lds<v4f>(L_QR + u * 256 + w * 32 + (l & 1) * 16);
    float M = mi[0];
    M = fmaxf(M, dpp<DPP_X1>(M)); M = fmaxf(M, dpp<DPP_X2>(M)); M = fmaxf(M, dpp<DPP_HMIR>(M));
    float L = __builtin_amdgcn_exp2f(mi[0] - M) * mi[1];
    L += dpp<DPP_X1>(L); L += dpp<DPP_X2>(L); L += dpp<DPP_HMIR>(L);
    float cs = (c[0] + c[1]) * __builtin_amdgcn_exp2f(mg[0] - M) + (c[2] + c[3]) * __builtin_amdgcn_exp2f(mg[2] - M);
    cs = tr16(cs, cs); cs = tr32(cs, cs);
    cs *= __builtin_amdgcn_rcpf(L);
    const u32 cu = canon(f2u(cs));
    v4u cv = {(u32)__builtin_amdgcn_mov_dpp(cu, 0x00, 0xF, 0xF, true), (u32)__builtin_amdgcn_mov_dpp(cu, 0x55, 0xF, 0xF, true),
              (u32)__builtin_amdgcn_mov_dpp(cu, 0xAA, 0xF, 0xF, true), (u32)__builtin_amdgcn_mov_dpp(cu, 0xFF, 0xF, 0xF, true)};
    if (g == 1) cv = (v4u){canon(f2u(hs[0])), canon(f2u(hs[1])), canon(f2u(hs[2])), canon(f2u(hs[3]))};
    if ((l < 16 && (l & 3) == 0) || (l >> 1) == 8) stc(cv, rws, l < 16 ? l * 4 : (l - 12) * 16, WS_CX + h * 8192 + w * 1024 + j * 128);
  }
  STAMP(11);
  if (w < 5) {
    __builtin_amdgcn_s_sleep(DL4);
    v4u cx; v2u hx;
    _Pragma("unroll 1") for (u32 it = 0; it < SPIN_CAP; ++it) {
      CLOB();
      cx = ldc(rws, (l & 7) * 128 + g * 16, WS_CX + h * 8192 + j * 1024);
      hx = ldc2(rws, (l & 7) * 128 + g * 8 + 64, WS_CX + h * 8192 + j * 1024);
      if (__all(max3u(max4u(cx), hx[0], hx[1]) != SENT)) break;
      __builtin_amdgcn_s_sleep(SL4);
    }
    STAMP(12);
    float a[8] = {0.f, 0.f, 0.f, 0.f, 0.f, 0.f, 0.f, 0.f};
#pragma unroll
    for (int t = 0; t < 4; ++t) {
      float c = u2f(cx[t]);
      c += dpp<DPP_X1>(c); c += dpp<DPP_X2>(c); c += dpp<DPP_HMIR>(c);
      fma8(a, ow[t], rbf(c));
    }
    float b[8] = {0.f, 0.f, 0.f, 0.f, 0.f, 0.f, 0.f, 0.f};
#pragma unroll
    for (int t = 0; t < 2; ++t) {
      float c = u2f(hx[t]);
      c += dpp<DPP_X1>(c); c += dpp<DPP_X2>(c); c += dpp<DPP_HMIR>(c);
      fma8(b, a2[t], fmaxf(rbf(c), 0.f));
    }
    float s0, s1, y0, y1;
    red8(a, s0, s1);
    red8(b, y0, y1);
    const u32 sp = canon(pk2(s0, s1)), yp = canon(pk2(y0, y1));
    auto xs = __builtin_amdgcn_permlane16_swap(sp, sp, false, false);
    auto xy = __builtin_amdgcn_permlane16_swap(yp, yp, false, false);
    const u32 se = xs[0], so = xs[1], ye = xy[0], yo = xy[1];
    if ((g & 1) == 0) stc((v4u){se, ye, so, yo}, rws, l15 * 8192 + (g >> 1) * 16, WS_F + w * 16 * 8192 + bi * 32);
    ldsst(L_CNT + 20, 1u);
    STAMP(13);
  } else if (w == 5 && bi < 80) {
    const u32 me = 8 * bi + 4 * (l >> 5) + (l & 1) + ((l >> 1) & 2);
    const u32 xv = ldx(rx, me);
    const u32 one = (l15 < 8) ? (0x3F80u << (16 * (l & 1))) : 0u;
    const v4u sel = {(l15 >> 1) == 0 ? one : 0u, (l15 >> 1) == 1 ? one : 0u, (l15 >> 1) == 2 ? one : 0u, (l15 >> 1) == 3 ? one : 0u};
    wait_ge(L_CNT + 20, 1u);
    __builtin_amdgcn_s_sleep(DL5);
    v4u f[8];
    _Pragma("unroll 1") for (u32 it = 0; it < SPIN_CAP; ++it) {
      CLOB();
      u32 mm = 0;
#pragma unroll
      for (int t = 0; t < 8; ++t) f[t] = ldc(rws, l * 16 + t * 1024, WS_F + bi * 8192);
#pragma unroll
      for (int t = 0; t < 8; ++t) mm = max3u(max3u(mm, f[t][0], f[t][1]), f[t][2], f[t][3]);
      if (__all(mm != SENT)) break;
      __builtin_amdgcn_s_sleep(SL5);
    }
    STAMP(12);
    v4f d0 = {0.f, 0.f, 0.f, 0.f}, d1 = {0.f, 0.f, 0.f, 0.f};
#pragma unroll
    for (int t = 0; t < 4; ++t) { d0 = mfma(f[2 * t], sel, d0); d1 = mfma(f[2 * t + 1], sel, d1); }
    d0 += d1;
    float v = tr32(d0[0] + d0[2], d0[1] + d0[3]);
    v = rbf(tr16(v, v));
    const float yv = dpp<0xEE>(v);
    const float gt = rbf(yv / (1.f + __builtin_amdgcn_exp2f(-1.4426950408889634f * yv)));
    const u32 ob = pk2(lo2f(xv) + rbf(v * gt), 0.f);
    if ((l & 0x1A) == 0) out[me] = (u16)ob;
    if (bi == 0 && l == 0) __builtin_amdgcn_raw_buffer_store_b32(tag, rws, 0, WS_EPOCH, 16);
    STAMP(13);
  }
}

extern "C" void kernel_launch(void* const* d_in, const int* in_sizes, int n_in, void* d_out, int out_size, void* d_ws, size_t ws_size,
                              hipStream_t stream) {
  (void)in_sizes; (void)n_in; (void)out_size; (void)ws_size;
  (void)hipFuncSetAttribute((const void*)mla_ffn_kernel, hipFuncAttributeMaxDynamicSharedMemorySize, L_TOTAL);
  mla_ffn_kernel<<<256, 512, L_TOTAL, stream>>>((const u16*)d_in[0], (const u16*)d_in[1], (const u16*)d_in[2], (const u16*)d_in[3],
                                                (const u16*)d_in[4], (const u16*)d_in[5], (const u16*)d_in[7], (const u16*)d_in[8],
                                                (u16*)d_out, (char*)d_ws);
}
```

```cpp
#include <hip/hip_runtime.h>
#include <hip/hip_bf16.h>
#include <stdint.h>

typedef unsigned short u16;
typedef unsigned int u32;
typedef short v4s __attribute__((ext_vector_type(4)));
typedef __bf16 v8bf __attribute__((ext_vector_type(8)));
typedef __bf16 v2bf __attribute__((ext_vector_type(2)));
typedef float v4f __attribute__((ext_vector_type(4)));
typedef float v2f __attribute__((ext_vector_type(2)));
typedef u32 v4u __attribute__((ext_vector_type(4)));
typedef u32 v2u __attribute__((ext_vector_type(2)));
#define LAS __attribute__((address_space(3)))
typedef __amdgpu_buffer_rsrc_t rsrc_t;

#define W_AUX 0
#define KV_NT ""
#define W1A 12
#define W1B 16
#define W0A 16
#define H0ROWS 24
#define AROWS 32
#define START0 1
#define OW_EARLY 0
#define DL1 8
#define SL1 1
#define DLG0 10
#define DLG1 40
#define SLG 2
#define DL2 10
#define SL2 4
#define DLM 10
#define SL3 2
#define DL3 0
#define DL4 6
#define SL4 1
#define SL5 2
#define DL5 10

constexpr u32 WS_EPOCH = 0;
constexpr u32 WS_HINT = 4096;
constexpr u32 WS_Q1 = WS_HINT + 8192;
constexpr u32 WS_QA = WS_Q1 + 65536;
constexpr u32 WS_ML = WS_QA + 32768;
constexpr u32 WS_R = WS_ML + 65536;
constexpr u32 R_F = 131072, R_SZ = R_F + 80 * 4096;
constexpr u32 WS_OP = WS_R + 2 * R_SZ;
constexpr u32 WS_END = WS_OP + 256 * 16384;
constexpr u32 WS_STAMP = WS_END;
static_assert(R_SZ == 128 * 3584, "clear split");

constexpr u32 RS = 1040;
constexpr u32 L_KV = 0;
constexpr u32 L_WK = 56 * RS;
constexpr u32 L_WQ = 88 * RS;
constexpr u32 L_UV = 0, L_W1 = 16384;
constexpr u32 L_QA = 128 * RS;
constexpr u32 L_P = L_QA + 16 * RS;
constexpr u32 L_X = L_P + 4096;
constexpr u32 L_MX = L_X + 1344;
constexpr u32 L_OA = L_MX + 256;
constexpr u32 L_ML = L_OA + 16 * 144;
constexpr u32 L_CNT = L_ML + 64;
constexpr u32 L_TOTAL = L_CNT + 64;
static_assert(L_WK + 32768 <= L_WQ && L_WQ + 40960 <= L_QA && L_W1 + 10240 <= 32 * RS, "staging");
static_assert(L_TOTAL <= 163840, "lds");

constexpr u32 SENT = 0xFFFFFFFFu;
constexpr u32 SPIN_CAP = 1u << 18;

__device__ __forceinline__ float u2f(u32 u) { return __uint_as_float(u); }
__device__ __forceinline__ u32 f2u(float f) { return __float_as_uint(f); }
__device__ __forceinline__ float lo2f(u32 u) { return __uint_as_float(u << 16); }
__device__ __forceinline__ float hi2f(u32 u) { return __uint_as_float(u & 0xFFFF0000u); }
__device__ __forceinline__ u32 pk2(float a, float b) { v2f f = {a, b}; v2bf c = __builtin_convertvector(f, v2bf); return *(u32*)&c; }
__device__ __forceinline__ float rbf(float a) { return lo2f(pk2(a, a)); }
__device__ __forceinline__ u32 canon(u32 u) { return u < 0xFFFFFFFEu ? u : 0xFFFFFFFEu; }
__device__ __forceinline__ u32 max3u(u32 a, u32 b, u32 c) { u32 m = a > b ? a : b; return m > c ? m : c; }
__device__ __forceinline__ u32 max4u(v4u v) { return max3u(max3u(v[0], v[1], v[2]), v[3], v[3]); }
template <int CTRL> __device__ __forceinline__ float dpp(float v) { return u2f(__builtin_amdgcn_mov_dpp(f2u(v), CTRL, 0xF, 0xF, true)); }
constexpr int DPP_X1 = 0xB1, DPP_X2 = 0x4E, DPP_ROR4 = 0x124, DPP_ROR8 = 0x128, DPP_HMIR = 0x141;
__device__ __forceinline__ float sum16(float v) { v += dpp<DPP_X1>(v); v += dpp<DPP_X2>(v); v += dpp<DPP_ROR4>(v); v += dpp<DPP_ROR8>(v); return v; }
__device__ __forceinline__ float max16(float v) { v = fmaxf(v, dpp<DPP_X1>(v)); v = fmaxf(v, dpp<DPP_X2>(v)); v = fmaxf(v, dpp<DPP_ROR4>(v)); v = fmaxf(v, dpp<DPP_ROR8>(v)); return v; }
__device__ __forceinline__ float tr16(float a, float b) { auto r = __builtin_amdgcn_permlane16_swap(f2u(a), f2u(b), false, false); u32 x = r[0], y = r[1]; return u2f(x) + u2f(y); }
__device__ __forceinline__ float tr32(float a, float b) { auto r = __builtin_amdgcn_permlane32_swap(f2u(a), f2u(b), false, false); u32 x = r[0], y = r[1]; return u2f(x) + u2f(y); }
__device__ __forceinline__ float mx16(float a) { auto r = __builtin_amdgcn_permlane16_swap(f2u(a), f2u(a), false, false); u32 x = r[0], y = r[1]; return fmaxf(u2f(x), u2f(y)); }
__device__ __forceinline__ float mx32(float a) { auto r = __builtin_amdgcn_permlane32_swap(f2u(a), f2u(a), false, false); u32 x = r[0], y = r[1]; return fmaxf(u2f(x), u2f(y)); }
__device__ __forceinline__ float sum64(float v) { v = sum16(v); v = tr16(v, v); return tr32(v, v); }
__device__ __forceinline__ float max64(float v) { v = max16(v); v = mx16(v); return mx32(v); }
__device__ __forceinline__ float dot2(u32 a, u32 b, float c) { return __builtin_amdgcn_fdot2_f32_bf16(*(v2bf*)&a, *(v2bf*)&b, c, false); }
__device__ __forceinline__ v4f mfma(v4u a, v4u b, v4f c) { return __builtin_amdgcn_mfma_f32_16x16x32_bf16(*(v8bf*)&a, *(v8bf*)&b, c, 0, 0, 0); }
__device__ __forceinline__ rsrc_t mk(const void* p, u32 n) { return __builtin_amdgcn_make_buffer_rsrc((void*)p, 0, n, 0x00020000); }
__device__ __forceinline__ v4u ldw(rsrc_t r, u32 v, u32 s) { return __builtin_amdgcn_raw_buffer_load_b128(r, v, s, W_AUX); }
__device__ __forceinline__ v4u ldc(rsrc_t r, u32 v, u32 s) { return __builtin_amdgcn_raw_buffer_load_b128(r, v, s, 16); }
__device__ __forceinline__ v2u ldc2(rsrc_t r, u32 v, u32 s) { return __builtin_amdgcn_raw_buffer_load_b64(r, v, s, 16); }
__device__ __forceinline__ void stc(v4u d, rsrc_t r, u32 v, u32 s) {
  __builtin_amdgcn_raw_buffer_store_b128(d, r, v, s, 16);
  asm volatile("s_nop 1" :: "v"(d));
}
__device__ __forceinline__ void stc2(v2u d, rsrc_t r, u32 v, u32 s) { __builtin_amdgcn_raw_buffer_store_b64(d, r, v, s, 16); }
__device__ __forceinline__ u16 ldx(rsrc_t r, u32 elem) { return __builtin_amdgcn_raw_buffer_load_b16(r, elem * 2, 0, 0); }
#define BAR() asm volatile("s_waitcnt lgkmcnt(0)\n\ts_barrier" ::: "memory")
#define SCHED() __builtin_amdgcn_sched_barrier(0)
#define CLOB() asm volatile("" ::: "memory")
#define CVM0() __builtin_amdgcn_s_waitcnt(0x0F70)
#define VM0() asm volatile("s_waitcnt vmcnt(0)" ::: "memory")
template <typename T> __device__ __forceinline__ LAS T* lds(u32 off) { return (LAS T*)(uintptr_t)off; }
__device__ __forceinline__ void fma8(float* a, v4u w, float x) {
#pragma unroll
  for (int e = 0; e < 4; ++e) { a[2 * e] = fmaf(lo2f(w[e]), x, a[2 * e]); a[2 * e + 1] = fmaf(hi2f(w[e]), x, a[2 * e + 1]); }
}
__device__ __forceinline__ void red8(const float* a, float& o0, float& o1) {
  float t0 = tr32(a[0], a[4]), t1 = tr32(a[1], a[5]), t2 = tr32(a[2], a[6]), t3 = tr32(a[3], a[7]);
  o0 = tr16(t0, t2); o1 = tr16(t1, t3);
}

__device__ __forceinline__ u32 ldsld(u32 off) { return __hip_atomic_load(lds<u32>(off), __ATOMIC_RELAXED, __HIP_MEMORY_SCOPE_WORKGROUP); }
__device__ __forceinline__ void ldsst(u32 off, u32 v) { __hip_atomic_store(lds<u32>(off), v, __ATOMIC_RELAXED, __HIP_MEMORY_SCOPE_WORKGROUP); }
__device__ __forceinline__ void wait_ge(u32 off, u32 v) {
  _Pragma("unroll 1") for (u32 it = 0; it < SPIN_CAP * 16; ++it) {
    if ((u32)__builtin_amdgcn_readfirstlane(ldsld(off)) >= v) break;
  }
  CLOB();
}
__device__ __forceinline__ u32 kv_prog() {
  CLOB();
  const v2u a = *(volatile LAS v2u*)lds<v2u>(L_CNT + 8);
  const u32 a0 = a[0], a1 = a[1];
  return __builtin_amdgcn_readfirstlane(a0 < a1 ? a0 : a1);
}
__device__ __forceinline__ void wait_kv(u32 v) {
  _Pragma("unroll 1") for (u32 it = 0; it < SPIN_CAP * 16; ++it) {
    if (kv_prog() >= v) break;
  }
  CLOB();
}
template <int W> __device__ __forceinline__ void dma_row(const char* src, u32 voff, u32 dst) {
  asm volatile("s_waitcnt vmcnt(%3)\n\ts_mov_b32 m0, %2\n\ts_nop 0\n\tglobal_load_lds_dwordx4 %0, %1" KV_NT
               :: "v"(voff), "s"(src), "s"(dst), "n"(W - 1) : "memory");
}
__device__ __forceinline__ void dma(const char* src, u32 voff, u32 dst) {
  asm volatile("s_mov_b32 m0, %2\n\ts_nop 0\n\tglobal_load_lds_dwordx4 %0, %1" :: "v"(voff), "s"(src), "s"(dst) : "memory");
}
__device__ __forceinline__ v4u trd(u32 a0, u32 a1) {
  v4s r0 = __builtin_amdgcn_ds_read_tr16_b64_v4i16(lds<v4s>(a0)), r1 = __builtin_amdgcn_ds_read_tr16_b64_v4i16(lds<v4s>(a1));
  v2u b0 = *(v2u*)&r0, b1 = *(v2u*)&r1;
  return (v4u){b0[0], b0[1], b1[0], b1[1]};
}
__device__ __forceinline__ void lds_inc(u32 off) {
  asm volatile("s_waitcnt lgkmcnt(0)" ::: "memory");
  if ((threadIdx.x & 63) == 0) __hip_atomic_fetch_add(lds<u32>(off), 1u, __ATOMIC_RELAXED, __HIP_MEMORY_SCOPE_WORKGROUP);
}
typedef unsigned short v2h __attribute__((ext_vector_type(2)));
__device__ __forceinline__ u32 pksub(u32 a, u32 b) { v2h r = __builtin_elementwise_sub_sat(*(v2h*)&a, *(v2h*)&b); return *(u32*)&r; }
constexpr v4f Z4 = {0.f, 0.f, 0.f, 0.f};

#define STAMP(k) do {} while (0)

__global__ void __launch_bounds__(512, 2)
mla_ffn_kernel(const u16* x, const u16* ckv, const u16* wq, const u16* wuk,
               const u16* wuv, const u16* wo, const u16* w1, const u16* w2,
               u16* out, char* ws) {
  extern __shared__ __attribute__((aligned(16))) char smem_[];
  STAMP(0);
  asm volatile("" :: "s"(x), "s"(ckv), "s"(wq), "s"(wuk), "s"(wuv), "s"(wo), "s"(w1), "s"(w2), "s"(out), "s"(ws));
  u32 tag;
  asm volatile("s_load_dword %0, %1, 0x0" : "=s"(tag) : "s"(ws) : "memory");
  const u32 bi = blockIdx.x, xc = bi & 7, r = bi >> 3;
  const u32 type = r & 1, pi = r >> 1;
  const u32 chunk = xc * 16 + pi;
#define MAPT 1
#define MAPQ 1
  const u32 h = MAPT ? (1 - type) * 16 + pi : (1 - type) * 16 + xc * 2 + (pi >> 3), j = MAPT ? xc : pi & 7;
  const u32 hq = MAPQ ? pi + 16 * (xc & 1) : xc * 4 + (pi >> 2), p = MAPQ ? xc >> 1 : pi & 3;
  const u32 w = __builtin_amdgcn_readfirstlane(threadIdx.x >> 6), l = threadIdx.x & 63;
  const u32 l15 = l & 15, g = l >> 4, q4 = l15 >> 2, pp = l & 3;
  const rsrc_t rws = mk(ws, WS_END);
  const v4u sv = {SENT, SENT, SENT, SENT};
  const char* kvsrc = (const char*)ckv + (size_t)chunk * 131072;
  const u32 swz = (l15 ^ (2 * g + 4 * (w & 2))) * 16;
  const u32 trq = 2 * g * 1024 + q4 * 256 + (((2 * w + (pp >> 1)) ^ (2 * q4 + 8 * (g & 1))) * 16) + (pp & 1) * 8;

  v4u xa; u32 xb;
  if (w == 4) asm volatile("global_load_dwordx4 %0, %2, %4\n\tglobal_load_dword %1, %3, %4 offset:1024" : "=&v"(xa), "=&v"(xb) : "v"(l * 16), "v"(l * 4), "s"(x) : "memory");
  if (w == 5) {
    if (type == 0 && l < 16) stc(sv, rws, l * 16, WS_QA + hq * 1024 + p * 256);
    if (l < 8) *lds<u32>(L_CNT + l * 4) = 0u;
  }
  if (type == 0) {
    const char* s0 = (const char*)wq + (160 * p + 4 * w) * 8192 + hq * 256;
#pragma unroll
    for (int i = 0; i < 5; ++i) dma(s0 + i * 32 * 8192, g * 8192 + swz, L_WQ + (w + 8 * i) * 1024);
    if (w < 4) {
      const char* s1 = (const char*)wuk + (128 * p + 32 * w) * 8192 + hq * 256;
#pragma unroll
      for (int i = 0; i < 8; ++i) dma(s1 + i * 4 * 8192, g * 8192 + ((l15 ^ g ^ (4 * (i & 3))) * 16), L_WK + w * 8192 + i * 1024);
    }
  } else {
    if (w >= 6) {
#pragma unroll 1
      for (u32 n = 0; n < W1A; ++n) { const u32 slot = 2 * n + w - 6; dma(kvsrc + slot * 1024, l * 16, slot * RS); }
    }
    BAR();
  }
  if (w == 4) {
    if (type) VM0();
    asm volatile("s_waitcnt vmcnt(5)" : "+v"(xa), "+v"(xb) :: "memory");
    float f[10];
#pragma unroll
    for (int k = 0; k < 4; ++k) { const u32 t = xa[k]; f[2 * k] = lo2f(t); f[2 * k + 1] = hi2f(t); }
    f[8] = lo2f(xb); f[9] = hi2f(xb);
    float s = 0.f;
#pragma unroll
    for (int k = 0; k < 10; ++k) s += f[k];
    const float mean = sum64(s) * (1.f / 640.f);
    float v = 0.f;
#pragma unroll
    for (int k = 0; k < 10; ++k) { const float a = f[k] - mean; v = fmaf(a, a, v); }
    const float mb = rbf(mean);
    *lds<v4u>(L_X + l * 16) = (v4u){pk2(f[0] - mb, f[1] - mb), pk2(f[2] - mb, f[3] - mb), pk2(f[4] - mb, f[5] - mb), pk2(f[6] - mb, f[7] - mb)};
    *lds<u32>(L_X + 1024 + l * 4) = pk2(f[8] - mb, f[9] - mb);
    v = rbf(sum64(v) * (1.f / 640.f));
    const float rs = __builtin_amdgcn_rcpf(rbf(__builtin_amdgcn_sqrtf(rbf(v + 1e-5f))));
    if (l == 0) *lds<v2f>(L_X + 1280) = (v2f){rs * (0.08838834764831845f * 1.4426950408889634f), rs};
    STAMP(14);
  }
  if (type == 0) {
    if (w < 4) asm volatile("s_waitcnt vmcnt(8)" ::: "memory"); else VM0();
    BAR();
    STAMP(1);
    v4u a[5], b[5];
#pragma unroll
    for (int s = 0; s < 5; ++s) { a[s] = trd(L_WQ + trq + s * 8192, L_WQ + trq + s * 8192 + 1024); b[s] = *lds<v4u>(L_X + p * 320 + s * 64 + g * 16); }
    const float rs = *lds<float>(L_X + 1280);
    v4f d = Z4;
#pragma unroll
    for (int s = 0; s < 5; ++s) d = mfma(a[s], b[s], d);
    asm volatile("" : "+s"(tag));
    if (l15 == 0) stc((v4u){pk2(d[0] * rs, d[1] * rs), tag + 1u, pk2(d[2] * rs, d[3] * rs), tag + 1u}, rws, g * 16, WS_Q1 + hq * 2048 + p * 512 + w * 64);
    if (w == 5 && l < 8) stc2((v2u){tag + 1u, tag + 1u}, rws, l * 1024, WS_HINT + (hq * 4 + p) * 8);
    if (w >= 4) lds_inc(L_CNT);
    STAMP(2);
  }
  asm volatile("" : "+s"(tag));
  tag += 1u;

  if (w < 4) {
    if (type == 0) {
      __builtin_amdgcn_s_sleep(DL1);
      v4u pq[2];
      CLOB();
#pragma unroll
      for (int k = 0; k < 2; ++k) pq[k] = ldc(rws, l15 * 128 + g * 32 + k * 16, WS_Q1 + hq * 2048);
      if (START0 == 1) ldsst(L_CNT + 4, 1u);
      asm volatile("s_waitcnt vmcnt(3)" ::: "memory");
      v4u kf[2][4];
#pragma unroll
      for (int s = 0; s < 4; ++s)
#pragma unroll
        for (int T = 0; T < 2; ++T) kf[T][s] = *lds<v4u>(L_WK + w * 8192 + T * 4096 + l15 * 256 + (((4 * s + g) ^ l15) * 16));
      lds_inc(L_CNT);
      _Pragma("unroll 1") for (u32 it = 0; it < SPIN_CAP; ++it) {
        if (__all(((pq[0][1] ^ tag) | (pq[0][3] ^ tag) | (pq[1][1] ^ tag) | (pq[1][3] ^ tag)) == 0)) break;
        __builtin_amdgcn_s_sleep(SL1);
        CLOB();
#pragma unroll
        for (int k = 0; k < 2; ++k) pq[k] = ldc(rws, l15 * 128 + g * 32 + k * 16, WS_Q1 + hq * 2048);
      }
      STAMP(3);
      const v4u qf = {pq[0][0], pq[0][2], pq[1][0], pq[1][2]};
      float e[2];
#pragma unroll
      for (int T = 0; T < 2; ++T) {
        const v4f d0 = mfma(qf, kf[T][0], Z4), d1 = mfma(qf, kf[T][1], Z4), d2 = mfma(qf, kf[T][2], Z4), d3 = mfma(qf, kf[T][3], Z4);
        e[T] = (d0[0] + d1[1]) + (d2[2] + d3[3]);
      }
      float v = tr32(e[0], e[1]);
      v = tr16(v, v);
      const u32 pk = pk2(v, dpp<DPP_X1>(v));
      if ((l & 17) == 0) __builtin_amdgcn_raw_buffer_store_b32(pk, rws, l15 * 2 + (l & 32), WS_QA + hq * 1024 + p * 256 + w * 64, 16);
      ldsst(L_CNT + 4, 2u);
      STAMP(4);
    }
    v4u af[2][16], pa[4];
    u32 sqa = WS_QA + type * 16384 + w * 4096, vqa = l * 16;
    asm volatile("" : "+s"(sqa), "+v"(vqa));
    u32 early = 0;
    _Pragma("unroll 1") for (u32 it = 0; it < SPIN_CAP * 16; ++it) {
      early = __builtin_amdgcn_readfirstlane(ldsld(L_CNT + 16));
      if (early || kv_prog() > w) break;
      __builtin_amdgcn_s_sleep(1);
    }
    CLOB();
    if (early) {
      STAMP(15);
#pragma unroll
      for (int k = 0; k < 4; ++k) pa[k] = ldc(rws, vqa + k * 1024, sqa);
      wait_kv(w + 1);
    }
    STAMP(20);
    {
      const u32 ab = L_KV + (32 * w + l15) * RS + g * 256;
#pragma unroll
      for (int k = 0; k < 16; ++k)
#pragma unroll
        for (int T = 0; T < 2; ++T) af[T][k] = *lds<v4u>(ab + T * 16 * RS + k * 16);
    }
    if (!early) {
      wait_ge(L_CNT + 16, 1u);
      STAMP(15);
#pragma unroll
      for (int k = 0; k < 4; ++k) pa[k] = ldc(rws, vqa + k * 1024, sqa);
    }
    _Pragma("unroll 1") for (u32 it = 0; it < SPIN_CAP; ++it) {
      u32 mm = 0;
#pragma unroll
      for (int k = 0; k < 4; ++k) {
        mm = max3u(max3u(mm, pa[k][0], pa[k][1]), pa[k][2], pa[k][3]);
        *lds<v4u>(L_QA + (4 * w + k) * RS + l * 16) = pa[k];
      }
      if (__all(mm != SENT)) break;
      __builtin_amdgcn_s_sleep(SL2);
      CLOB();
#pragma unroll
      for (int k = 0; k < 4; ++k) pa[k] = ldc(rws, vqa + k * 1024, sqa);
    }
    STAMP(5);
    BAR();
    STAMP(6);
    v4f s0 = Z4, s1 = Z4;
    {
      const u32 bb = L_QA + l15 * RS + g * 256;
      v4u bq[16];
#pragma unroll
      for (int s = 0; s < 16; ++s) bq[s] = *lds<v4u>(bb + s * 16);
      SCHED();
#pragma unroll
      for (int s = 0; s < 16; ++s) { s0 = mfma(af[0][s], bq[s], s0); s1 = mfma(af[1][s], bq[s], s1); }
    }
    float mx = fmaxf(fmaxf(fmaxf(s0[0], s0[1]), fmaxf(s0[2], s0[3])), fmaxf(fmaxf(s1[0], s1[1]), fmaxf(s1[2], s1[3])));
    mx = __builtin_ceilf(mx32(mx16(mx)));
    if (g == 0) *lds<float>(L_MX + l15 * 16 + w * 4) = mx;
#define EX(v) __builtin_amdgcn_exp2f((v) - mx)
    *lds<v4u>(L_P + w * 1024 + l * 16) = (v4u){pk2(EX(s0[0]), EX(s0[1])), pk2(EX(s0[2]), EX(s0[3])), pk2(EX(s1[0]), EX(s1[1])), pk2(EX(s1[2]), EX(s1[3]))};
    STAMP(17);
    BAR();
    STAMP(7);
    if (w == 0) {
      const v4f m4 = *lds<v4f>(L_MX + l15 * 16);
      const float M = fmaxf(fmaxf(m4[0], m4[1]), fmaxf(m4[2], m4[3]));
      float L = 0.f;
#pragma unroll
      for (int s = 0; s < 4; ++s) {
        const v4u pf = *lds<v4u>(L_P + s * 1024 + l * 16);
        float a = 0.f;
#pragma unroll
        for (int e = 0; e < 4; ++e) a = dot2(pf[e], 0x3F803F80u, a);
        L = fmaf(a, __builtin_amdgcn_exp2f(m4[s] - M), L);
      }
      L = tr16(L, L); L = tr32(L, L);
      if (g == 0) stc((v4u){f2u(M), tag, f2u(L), tag}, rws, l15 * 2048, WS_ML + type * 32768 + chunk * 16);
    }
    STAMP(8);
    __builtin_amdgcn_s_sleep(DLM);
  } else {
    const u32 v = w - 4;
    if (w == 4) {
      if (type == 0) wait_ge(L_CNT + 4, 2u);
#pragma unroll 1
      for (u32 k = 0; k < 16; ++k) stc(sv, rws, (l >> 3) * 16384 + (l & 7) * 16, WS_OP + (type * 16 + k) * 131072 + chunk * 128);
#pragma unroll
      for (int k = 0; k < 4; ++k) if (k < 3 || l < 32) stc(sv, rws, l * 16 + k * 1024, WS_R + (1 - type) * R_SZ + chunk * 3584);
      STAMP(3);
    } else if (w == 5) {
      if (type == 0) __builtin_amdgcn_s_sleep(DLG0); else __builtin_amdgcn_s_sleep(DLG1);
      _Pragma("unroll 1") for (u32 it = 0; it < SPIN_CAP; ++it) {
        CLOB();
        const v2u hv = ldc2(rws, l * 8, WS_HINT + xc * 1024 + type * 512);
        if (__all(hv[0] == tag && hv[1] == tag)) break;
        __builtin_amdgcn_s_sleep(SLG);
      }
      STAMP(3);
      __builtin_amdgcn_s_sleep(DL2);
      ldsst(L_CNT + 16, 1u);
    } else {
      const u32 lw = w - 6;
      if (type == 0) {
        if (START0) wait_ge(L_CNT + 4, START0);
        STAMP(4);
#pragma unroll 1
        for (u32 s = 0; s < 4; ++s) {
#pragma unroll 1
          for (u32 n = 0; n < 16; ++n) {
            const u32 slot = 32 * s + 2 * n + lw;
            if ((slot | 1) == 57) wait_ge(L_CNT, 8u);
            const u32 row = slot < H0ROWS ? slot : slot < H0ROWS + AROWS ? slot + (128 - AROWS - H0ROWS) : slot - AROWS;
            dma_row<W0A>(kvsrc + row * 1024, l * 16, slot * RS);
          }
          asm volatile("s_waitcnt vmcnt(16)" ::: "memory");
          ldsst(L_CNT + 8 + 4 * lw, s);
        }
      } else {
#pragma unroll 1
        for (u32 n = W1A; n < 32; ++n) {
          const u32 slot = 2 * n + lw;
          dma_row<W1A>(kvsrc + slot * 1024, l * 16, slot * RS);
          if (n == 15 + W1A) ldsst(L_CNT + 8 + 4 * lw, 1u);
        }
        STAMP(2);
#pragma unroll 1
        for (u32 s = 2; s < 4; ++s) {
#pragma unroll 1
          for (u32 n = 0; n < 16; ++n) {
            const u32 slot = 32 * s + 2 * n + lw;
            dma_row<W1B>(kvsrc + slot * 1024, l * 16, slot * RS);
          }
          asm volatile("s_waitcnt vmcnt(16)" ::: "memory");
          ldsst(L_CNT + 8 + 4 * lw, s);
        }
      }
      STAMP(3);
    }
    v4u tv[4][8];
    const u32 tb = L_KV + (4 * g + q4) * RS + v * 256 + (pp & 1) * 8 + (pp >> 1) * 128;
#pragma unroll
    for (int s = 0; s < 3; ++s) {
      wait_kv(s + 1);
#pragma unroll
      for (int t = 0; t < 8; ++t) tv[s][t] = trd(tb + 32 * s * RS + 16 * t, tb + (32 * s + 16) * RS + 16 * t);
      SCHED();
    }
    VM0();
    if (w >= 6) ldsst(L_CNT + 8 + 4 * (w - 6), 4u);
    STAMP(5);
    BAR();
    STAMP(6);
#pragma unroll
    for (int t = 0; t < 8; ++t) tv[3][t] = trd(tb + 96 * RS + 16 * t, tb + 112 * RS + 16 * t);
    {
      const char* s0 = (const char*)wuv + (64 * j + 32 * (v & 1) + (v >> 1)) * 8192 + h * 256;
#pragma unroll
      for (int i = 0; i < 4; ++i) dma(s0 + i * 2 * 8192, g * 65536 + swz, L_UV + (v + 4 * i) * 1024);
      if (h < 27) {
        const char* s1 = (const char*)w1 + (80 * j + 8 * v) * 3456 + h * 128;
#pragma unroll
        for (int i = 0; i < 3; ++i) if (i < 2 || v < 2) dma(s1 + i * 32 * 3456, (l >> 3) * 3456 + (l & 7) * 16, L_W1 + (v + 4 * i) * 1024);
      }
    }
    STAMP(17);
    BAR();
    STAMP(7);
    const v4f m4 = *lds<v4f>(L_MX + l15 * 16);
    v4u pf[4];
#pragma unroll
    for (int s = 0; s < 4; ++s) pf[s] = *lds<v4u>(L_P + s * 1024 + l * 16);
    const float M = fmaxf(fmaxf(m4[0], m4[1]), fmaxf(m4[2], m4[3]));
    v4f o[8];
#pragma unroll
    for (int s = 0; s < 4; ++s) {
      const u32 dl = (u32)fminf(M - m4[s], 255.f) * 0x800080u;
      const v4u ps = {pksub(pf[s][0], dl), pksub(pf[s][1], dl), pksub(pf[s][2], dl), pksub(pf[s][3], dl)};
#pragma unroll
      for (int t = 0; t < 8; ++t) o[t] = mfma(ps, tv[s][t], s ? o[t] : Z4);
    }
#pragma unroll
    for (int e = 0; e < 4; ++e)
      stc((v4u){pk2(o[0][e], o[1][e]), pk2(o[2][e], o[3][e]), pk2(o[4][e], o[5][e]), pk2(o[6][e], o[7][e])}, rws,
          g * 524288 + (l15 >> 3) * 16384 + (l & 7) * 16, WS_OP + (type * 128 + 8 * e + 2 * v) * 16384 + chunk * 128);
    STAMP(8);
  }

  const u32 rcx = WS_R + type * R_SZ + (h & 15) * 8192;
  v4u a2[2], ow[4];
  auto late = [&]() {
    const rsrc_t ro = mk(wo, w < 5 ? 4096 * 640 * 2 : 0);
#pragma unroll
    for (int t = 0; t < 4; ++t) ow[t] = ldw(ro, (4 * g + t) * 1280 + l15 * 16, (h * 128 + j * 16) * 1280 + w * 256);
  };
  if (OW_EARLY) late();
  float rl;
  v4u uf[2];
  {
    v4u op[2], ml[2];
    _Pragma("unroll 1") for (u32 it = 0; it < SPIN_CAP; ++it) {
      CLOB();
#pragma unroll
      for (int t = 0; t < 2; ++t) ml[t] = ldc(rws, (l >> 3) * 16 + t * 128, WS_ML + h * 2048 + w * 256);
      if (__all(((ml[0][1] ^ tag) | (ml[0][3] ^ tag) | (ml[1][1] ^ tag) | (ml[1][3] ^ tag)) == 0)) break;
      __builtin_amdgcn_s_sleep(SL3);
    }
    STAMP(16);
    __builtin_amdgcn_s_sleep(DL3);
    CLOB();
#pragma unroll
    for (int t = 0; t < 2; ++t) op[t] = ldc(rws, l * 16 + t * 1024, WS_OP + (h * 8 + j) * 16384 + w * 2048);
    SCHED();
    const float m0 = u2f(ml[0][0]), m1 = u2f(ml[1][0]);
    float M = fmaxf(m0, m1);
    M = fmaxf(M, dpp<DPP_ROR8>(M)); M = mx32(mx16(M));
    float L = __builtin_amdgcn_exp2f(m0 - M) * u2f(ml[0][2]) + __builtin_amdgcn_exp2f(m1 - M) * u2f(ml[1][2]);
    L += dpp<DPP_ROR8>(L); L = tr16(L, L); L = tr32(L, L);
    if (l == 0) *lds<v2f>(L_ML + w * 8) = (v2f){M, L};
    BAR();
    STAMP(9);
    if (!OW_EARLY) late();
    const rsrc_t r2 = mk(w2, h < 27 && w < 5 ? 1728 * 640 * 2 : 0);
#pragma unroll
    for (int t = 0; t < 2; ++t) a2[t] = ldw(r2, (2 * g + t) * 1280 + l15 * 16, (64 * h + 8 * j) * 1280 + w * 256);
    SCHED();
    const v2f mi = *lds<v2f>(L_ML + (l & 7) * 8);
#pragma unroll
    for (int s = 0; s < 2; ++s) uf[s] = trd(L_UV + trq + s * 8192, L_UV + trq + s * 8192 + 1024);
    M = mi[0];
    M = fmaxf(M, dpp<DPP_X1>(M)); M = fmaxf(M, dpp<DPP_X2>(M)); M = fmaxf(M, dpp<DPP_HMIR>(M));
    L = __builtin_amdgcn_exp2f(mi[0] - M) * mi[1];
    L += dpp<DPP_X1>(L); L += dpp<DPP_X2>(L); L += dpp<DPP_HMIR>(L);
    rl = __builtin_amdgcn_rcpf(L);
    u32 cp = pk2(__builtin_amdgcn_exp2f(m0 - M), __builtin_amdgcn_exp2f(m1 - M));
    {
      v4f d = Z4;
      if (w < 4 && h < 27) {
        const u32 ta = L_W1 + (8 * g + q4) * 128 + w * 32 + pp * 8;
#pragma unroll
        for (int s = 0; s < 3; ++s) {
          v4u xf = *lds<v4u>(L_X + j * 160 + s * 64 + g * 16);
          const u32 tt = (s == 2 && g >= 2) ? ta + 48 * 128 : ta + s * 32 * 128;
          if (s == 2 && g >= 2) xf = (v4u){0u, 0u, 0u, 0u};
          d = mfma(xf, trd(tt, tt + 512), d);
        }
      }
      const float hp = d[0] * *lds<float>(L_X + 1284);
      __builtin_amdgcn_raw_buffer_store_b32(f2u(hp), rws, threadIdx.x < 256 && l < 16 ? (l >> 3) * 1024 + (l & 7) * 4 : 0x40000000u, rcx + w * 2048 + j * 128 + 64, 16);
    }
    asm volatile("" : "+v"(cp), "+v"(rl), "+v"(uf[0]), "+v"(uf[1]));
    STAMP(18);
    if (!__all(max3u(max4u(op[0]), max4u(op[1]), 0u) != SENT)) {
      _Pragma("unroll 1") for (u32 it = 0; it < SPIN_CAP; ++it) {
        __builtin_amdgcn_s_sleep(SL3);
        CLOB();
#pragma unroll
        for (int t = 0; t < 2; ++t) op[t] = ldc(rws, l * 16 + t * 1024, WS_OP + (h * 8 + j) * 16384 + w * 2048);
        if (__all(max3u(max4u(op[0]), max4u(op[1]), 0u) != SENT)) break;
      }
    }
    STAMP(10);
    float a[8];
#pragma unroll
    for (int e = 0; e < 4; ++e) {
      const u32 u0 = op[0][e], u1 = op[1][e];
      a[2 * e] = dot2(__builtin_amdgcn_perm(u1, u0, 0x05040100u), cp, 0.f);
      a[2 * e + 1] = dot2(__builtin_amdgcn_perm(u1, u0, 0x07060302u), cp, 0.f);
    }
    float o0, o1;
    red8(a, o0, o1);
    *lds<u32>(L_OA + (2 * w + ((l >> 3) & 1)) * 144 + ((l & 7) * 8 + g * 2) * 2) = pk2(o0, o1);
  }
  BAR();
  STAMP(19);
  {
    v4f c = mfma(*lds<v4u>(L_OA + l15 * 144 + g * 16), uf[0], Z4);
    c = mfma(*lds<v4u>(L_OA + l15 * 144 + 64 + g * 16), uf[1], c);
    float cs = (c[0] + c[1]) + (c[2] + c[3]);
    cs = tr16(cs, cs); cs = tr32(cs, cs);
    if (l < 16) __builtin_amdgcn_raw_buffer_store_b32(f2u(cs * rl), rws, l * 4, rcx + w * 1024 + j * 128, 16);
  }
  STAMP(11);
  if (w < 5) {
    __builtin_amdgcn_s_sleep(DL4);
    v4u cx; v2u hx;
    _Pragma("unroll 1") for (u32 it = 0; it < SPIN_CAP; ++it) {
      CLOB();
      cx = ldc(rws, (l & 7) * 128 + g * 16, rcx + j * 1024);
      hx = ldc2(rws, (l & 7) * 128 + g * 8 + 64, rcx + j * 1024);
      if (__all(max3u(max4u(cx), hx[0], hx[1]) != SENT)) break;
      __builtin_amdgcn_s_sleep(SL4);
    }
    STAMP(12);
    float a[8] = {0.f, 0.f, 0.f, 0.f, 0.f, 0.f, 0.f, 0.f};
#pragma unroll
    for (int t = 0; t < 4; ++t) {
      float c = u2f(cx[t]);
      c += dpp<DPP_X1>(c); c += dpp<DPP_X2>(c); c += dpp<DPP_HMIR>(c);
      fma8(a, ow[t], c);
    }
    float b[8] = {0.f, 0.f, 0.f, 0.f, 0.f, 0.f, 0.f, 0.f};
#pragma unroll
    for (int t = 0; t < 2; ++t) {
      float c = u2f(hx[t]);
      c += dpp<DPP_X1>(c); c += dpp<DPP_X2>(c); c += dpp<DPP_HMIR>(c);
      fma8(b, a2[t], fmaxf(c, 0.f));
    }
    float s0, s1, y0, y1;
    red8(a, s0, s1);
    red8(b, y0, y1);
    const u32 sp = pk2(s0, s1), yp = pk2(y0, y1);
    auto xs = __builtin_amdgcn_permlane16_swap(sp, sp, false, false);
    auto xy = __builtin_amdgcn_permlane16_swap(yp, yp, false, false);
    const u32 se = xs[0], so = xs[1], ye = xy[0], yo = xy[1];
    if ((g & 1) == 0) stc((v4u){se, ye, so, yo}, rws, l15 * 4096 + (g >> 1) * 16, WS_R + type * R_SZ + R_F + w * 16 * 4096 + chunk * 32);
    ldsst(L_CNT + 20, 1u);
    STAMP(13);
  } else if (w == 5 && bi < 80) {
    const u32 me = 8 * bi + 4 * (l >> 5) + (l & 1) + ((l >> 1) & 2);
    const u32 xv = x[me];
    const u32 one = (l15 < 8) ? (0x3F80u << (16 * (l & 1))) : 0u;
    const v4u sel = {(l15 >> 1) == 0 ? one : 0u, (l15 >> 1) == 1 ? one : 0u, (l15 >> 1) == 2 ? one : 0u, (l15 >> 1) == 3 ? one : 0u};
    _Pragma("unroll 1") for (u32 it = 0; it < SPIN_CAP; ++it) {
      CLOB();
      const v2u t0 = ldc2(rws, l * 16, WS_ML + (h ^ 16) * 2048), t1 = ldc2(rws, l * 16 + 1024, WS_ML + (h ^ 16) * 2048);
      if (__all(t0[1] == tag && t1[1] == tag)) break;
      __builtin_amdgcn_s_sleep(SL5);
    }
    wait_ge(L_CNT + 20, 1u);
    __builtin_amdgcn_s_sleep(DL5);
    v4u f[8];
    _Pragma("unroll 1") for (u32 it = 0; it < SPIN_CAP; ++it) {
      CLOB();
      u32 mm = 0;
#pragma unroll
      for (int t = 0; t < 8; ++t) f[t] = ldc(rws, l * 16 + (t & 3) * 1024, WS_R + (t >> 2) * R_SZ + R_F + bi * 4096);
#pragma unroll
      for (int t = 0; t < 8; ++t) mm = max3u(max3u(mm, f[t][0], f[t][1]), f[t][2], f[t][3]);
      if (__all(mm != SENT)) break;
      __builtin_amdgcn_s_sleep(SL5);
    }
    STAMP(12);
    v4f d0 = Z4, d1 = Z4;
#pragma unroll
    for (int t = 0; t < 4; ++t) { d0 = mfma(f[2 * t], sel, d0); d1 = mfma(f[2 * t + 1], sel, d1); }
    d0 += d1;
    float v = tr32(d0[0] + d0[2], d0[1] + d0[3]);
    v = rbf(tr16(v, v));
    const float yv = dpp<0xEE>(v);
    const float gt = rbf(yv * __builtin_amdgcn_rcpf(1.f + __builtin_amdgcn_exp2f(-1.4426950408889634f * yv)));
    const u32 ob = pk2(lo2f(xv) + rbf(v * gt), 0.f);
    if ((l & 0x1A) == 0) out[me] = (u16)ob;
    if (bi == 0 && l == 0) __builtin_amdgcn_raw_buffer_store_b32(tag, rws, 0, WS_EPOCH, 16);
    STAMP(13);
  }
}

extern "C" void kernel_launch(void* const* d_in, const int* in_sizes, int n_in, void* d_out, int out_size, void* d_ws, size_t ws_size,
                              hipStream_t stream) {
  (void)in_sizes; (void)n_in; (void)out_size; (void)ws_size;
  (void)hipFuncSetAttribute((const void*)mla_ffn_kernel, hipFuncAttributeMaxDynamicSharedMemorySize, L_TOTAL);
  mla_ffn_kernel<<<256, 512, L_TOTAL, stream>>>((const u16*)d_in[0], (const u16*)d_in[1], (const u16*)d_in[2], (const u16*)d_in[3],
                                                (const u16*)d_in[4], (const u16*)d_in[5], (const u16*)d_in[7], (const u16*)d_in[8],
                                                (u16*)d_out, (char*)d_ws);
}
```

```cpp
#include <hip/hip_runtime.h>
#include <hip/hip_bf16.h>
#include <stdint.h>

typedef unsigned short u16;
typedef unsigned int u32;
typedef short v4s __attribute__((ext_vector_type(4)));
typedef __bf16 v8bf __attribute__((ext_vector_type(8)));
typedef __bf16 v2bf __attribute__((ext_vector_type(2)));
typedef float v4f __attribute__((ext_vector_type(4)));
typedef float v2f __attribute__((ext_vector_type(2)));
typedef u32 v4u __attribute__((ext_vector_type(4)));
typedef u32 v2u __attribute__((ext_vector_type(2)));
#define LAS __attribute__((address_space(3)))
typedef __amdgpu_buffer_rsrc_t rsrc_t;

#define W_AUX 0
#define KV_NT ""
#define N0 0
#define DS1 10
#define W1 5
#define W0 8
#define H0B 2
#define AB 0
#define DC1 60
#define DT 150
#define DPV 4
#define PFQ 0
#define PFK 0
#define UKPOS 0
#define DPK 0
#define WARM 0
#define PFC 1
#define OW_EARLY 0
#define DL1 8
#define SL1 1
#define DLG1 1
#define SLG 6
#define SL2 4
#define DLM 10
#define SL3 2
#define DL3 0
#define DL4 6
#define SL4 1
#define SL5 2
#define DL5 10

constexpr u32 WS_EPOCH = 0;
constexpr u32 WS_HINT = 4096;
constexpr u32 WS_Q1 = WS_HINT + 8192;
constexpr u32 WS_QA = WS_Q1 + 65536;
constexpr u32 WS_ML = WS_QA + 32768;
constexpr u32 WS_R = WS_ML + 65536;
constexpr u32 R_F = 131072, R_SZ = R_F + 80 * 4096;
constexpr u32 WS_OP = WS_R + 2 * R_SZ;
constexpr u32 WS_END = WS_OP + 256 * 16384;
constexpr u32 WS_STAMP = WS_END;
static_assert(R_SZ == 128 * 3584, "clear split");

constexpr u32 RS = 1040;
constexpr u32 L_KV = 0;
constexpr u32 L_WK = 64 * RS;
constexpr u32 L_WQ = L_WK + 32 * 1088;
constexpr u32 L_UV = 0, L_W1 = 16384;
constexpr u32 L_QA = 128 * RS;
constexpr u32 L_P = L_QA + 16 * RS;
constexpr u32 L_X = L_P + 4096;
constexpr u32 L_MX = L_X + 1344;
constexpr u32 L_OA = L_MX + 272;
constexpr u32 L_ML = L_OA + 16 * 144;
constexpr u32 L_CNT = L_ML + 64;
constexpr u32 L_TOTAL = L_CNT + 64;
static_assert(L_WQ + 40960 <= L_P && L_W1 + 10240 <= 32 * RS, "staging");
static_assert(L_TOTAL <= 163840, "lds");

constexpr u32 SENT = 0xFFFFFFFFu;
constexpr u32 SPIN_CAP = 1u << 18;

__device__ __forceinline__ float u2f(u32 u) { return __uint_as_float(u); }
__device__ __forceinline__ u32 f2u(float f) { return __float_as_uint(f); }
__device__ __forceinline__ float lo2f(u32 u) { return __uint_as_float(u << 16); }
__device__ __forceinline__ float hi2f(u32 u) { return __uint_as_float(u & 0xFFFF0000u); }
__device__ __forceinline__ u32 pk2(float a, float b) { v2f f = {a, b}; v2bf c = __builtin_convertvector(f, v2bf); return *(u32*)&c; }
__device__ __forceinline__ float rbf(float a) { return lo2f(pk2(a, a)); }
__device__ __forceinline__ u32 canon(u32 u) { return u < 0xFFFFFFFEu ? u : 0xFFFFFFFEu; }
__device__ __forceinline__ u32 max3u(u32 a, u32 b, u32 c) { u32 m = a > b ? a : b; return m > c ? m : c; }
__device__ __forceinline__ u32 max4u(v4u v) { return max3u(max3u(v[0], v[1], v[2]), v[3], v[3]); }
template <int CTRL> __device__ __forceinline__ float dpp(float v) { return u2f(__builtin_amdgcn_mov_dpp(f2u(v), CTRL, 0xF, 0xF, true)); }
constexpr int DPP_X1 = 0xB1, DPP_X2 = 0x4E, DPP_ROR4 = 0x124, DPP_ROR8 = 0x128, DPP_HMIR = 0x141;
__device__ __forceinline__ float sum16(float v) { v += dpp<DPP_X1>(v); v += dpp<DPP_X2>(v); v += dpp<DPP_ROR4>(v); v += dpp<DPP_ROR8>(v); return v; }
__device__ __forceinline__ float max16(float v) { v = fmaxf(v, dpp<DPP_X1>(v)); v = fmaxf(v, dpp<DPP_X2>(v)); v = fmaxf(v, dpp<DPP_ROR4>(v)); v = fmaxf(v, dpp<DPP_ROR8>(v)); return v; }
__device__ __forceinline__ float tr16(float a, float b) { auto r = __builtin_amdgcn_permlane16_swap(f2u(a), f2u(b), false, false); u32 x = r[0], y = r[1]; return u2f(x) + u2f(y); }
__device__ __forceinline__ float tr32(float a, float b) { auto r = __builtin_amdgcn_permlane32_swap(f2u(a), f2u(b), false, false); u32 x = r[0], y = r[1]; return u2f(x) + u2f(y); }
__device__ __forceinline__ float mx16(float a) { auto r = __builtin_amdgcn_permlane16_swap(f2u(a), f2u(a), false, false); u32 x = r[0], y = r[1]; return fmaxf(u2f(x), u2f(y)); }
__device__ __forceinline__ float mx32(float a) { auto r = __builtin_amdgcn_permlane32_swap(f2u(a), f2u(a), false, false); u32 x = r[0], y = r[1]; return fmaxf(u2f(x), u2f(y)); }
__device__ __forceinline__ float sum64(float v) { v = sum16(v); v = tr16(v, v); return tr32(v, v); }
__device__ __forceinline__ float max64(float v) { v = max16(v); v = mx16(v); return mx32(v); }
__device__ __forceinline__ float dot2(u32 a, u32 b, float c) { return __builtin_amdgcn_fdot2_f32_bf16(*(v2bf*)&a, *(v2bf*)&b, c, false); }
__device__ __forceinline__ v4f mfma(v4u a, v4u b, v4f c) { return __builtin_amdgcn_mfma_f32_16x16x32_bf16(*(v8bf*)&a, *(v8bf*)&b, c, 0, 0, 0); }
__device__ __forceinline__ rsrc_t mk(const void* p, u32 n) { return __builtin_amdgcn_make_buffer_rsrc((void*)p, 0, n, 0x00020000); }
__device__ __forceinline__ v4u ldw(rsrc_t r, u32 v, u32 s) { return __builtin_amdgcn_raw_buffer_load_b128(r, v, s, W_AUX); }
__device__ __forceinline__ v4u ldc(rsrc_t r, u32 v, u32 s) { return __builtin_amdgcn_raw_buffer_load_b128(r, v, s, 16); }
__device__ __forceinline__ v2u ldc2(rsrc_t r, u32 v, u32 s) { return __builtin_amdgcn_raw_buffer_load_b64(r, v, s, 16); }
__device__ __forceinline__ void stc(v4u d, rsrc_t r, u32 v, u32 s) {
  __builtin_amdgcn_raw_buffer_store_b128(d, r, v, s, 16);
  asm volatile("s_nop 1" :: "v"(d));
}
__device__ __forceinline__ void stc2(v2u d, rsrc_t r, u32 v, u32 s) { __builtin_amdgcn_raw_buffer_store_b64(d, r, v, s, 16); }
__device__ __forceinline__ u16 ldx(rsrc_t r, u32 elem) { return __builtin_amdgcn_raw_buffer_load_b16(r, elem * 2, 0, 0); }
#define BAR() asm volatile("s_waitcnt lgkmcnt(0)\n\ts_barrier" ::: "memory")
#define SCHED() __builtin_amdgcn_sched_barrier(0)
#define CLOB() asm volatile("" ::: "memory")
#define CVM0() __builtin_amdgcn_s_waitcnt(0x0F70)
#define VM0() asm volatile("s_waitcnt vmcnt(0)" ::: "memory")
template <typename T> __device__ __forceinline__ LAS T* lds(u32 off) { return (LAS T*)(uintptr_t)off; }
__device__ __forceinline__ void fma8(float* a, v4u w, float x) {
#pragma unroll
  for (int e = 0; e < 4; ++e) { a[2 * e] = fmaf(lo2f(w[e]), x, a[2 * e]); a[2 * e + 1] = fmaf(hi2f(w[e]), x, a[2 * e + 1]); }
}
__device__ __forceinline__ void red8(const float* a, float& o0, float& o1) {
  float t0 = tr32(a[0], a[4]), t1 = tr32(a[1], a[5]), t2 = tr32(a[2], a[6]), t3 = tr32(a[3], a[7]);
  o0 = tr16(t0, t2); o1 = tr16(t1, t3);
}

__device__ __forceinline__ u32 ldsld(u32 off) { return __hip_atomic_load(lds<u32>(off), __ATOMIC_RELAXED, __HIP_MEMORY_SCOPE_WORKGROUP); }
__device__ __forceinline__ void ldsst(u32 off, u32 v) { __hip_atomic_store(lds<u32>(off), v, __ATOMIC_RELAXED, __HIP_MEMORY_SCOPE_WORKGROUP); }
__device__ __forceinline__ void wait_ge(u32 off, u32 v) {
  _Pragma("unroll 1") for (u32 it = 0; it < SPIN_CAP * 16; ++it) {
    if ((u32)__builtin_amdgcn_readfirstlane(ldsld(off)) >= v) break;
  }
  CLOB();
}
__device__ __forceinline__ void wait_kv(u32 v) {
  _Pragma("unroll 1") for (u32 it = 0; it < SPIN_CAP * 16; ++it) {
    CLOB();
    const v4u a = *(volatile LAS v4u*)lds<v4u>(L_CNT + 16);
    const u32 m = min(min(a[0], a[1]), min(a[2], a[3]));
    if ((u32)__builtin_amdgcn_readfirstlane(m) >= v) break;
  }
  CLOB();
}
template <int W, int SLOT> __device__ __forceinline__ void kv_row(const char* src, u32 voff, u32 lb) {
  asm volatile("s_add_u32 m0, %2, %3\n\ts_waitcnt vmcnt(%4)\n\tglobal_load_lds_dwordx4 %0, %1 offset:%5" KV_NT
               :: "v"(voff), "s"(src), "s"(lb), "n"(SLOT * RS - (SLOT & 3) * 1024), "n"(W - 1), "n"((SLOT & 3) * 1024) : "memory", "scc");
}
template <int W, int B, int FROM = 0, int TO = 4> __device__ __forceinline__ void kv_blk(const char* src, u32 voff, u32 lb) {
  if (FROM <= 0 && TO > 0) kv_row<W, 16 * B>(src, voff, lb);
  if (FROM <= 1 && TO > 1) kv_row<W, 16 * B + 1>(src, voff, lb);
  if (FROM <= 2 && TO > 2) kv_row<W, 16 * B + 2>(src, voff, lb);
  if (FROM <= 3 && TO > 3) kv_row<W, 16 * B + 3>(src, voff, lb);
}
template <int N, int GS, int LS> __device__ __forceinline__ void dma_run(const char* src, u32 voff, u32 dst) {
  asm volatile("s_mov_b32 m0, %2\n\ts_nop 0\n\tglobal_load_lds_dwordx4 %0, %1\n\t"
               ".rept %3\n\ts_add_u32 m0, m0, %5\n\tv_add_u32 %0, %4, %0\n\tglobal_load_lds_dwordx4 %0, %1\n\t.endr"
               : "+v"(voff) : "s"(src), "s"(dst), "n"(N - 1), "n"(GS), "n"(LS) : "memory", "scc");
}
__device__ __forceinline__ void dma(const char* src, u32 voff, u32 dst) {
  asm volatile("s_mov_b32 m0, %2\n\ts_nop 0\n\tglobal_load_lds_dwordx4 %0, %1" :: "v"(voff), "s"(src), "s"(dst) : "memory");
}
__device__ __forceinline__ v4u trd(u32 a0, u32 a1) {
  v4s r0 = __builtin_amdgcn_ds_read_tr16_b64_v4i16(lds<v4s>(a0)), r1 = __builtin_amdgcn_ds_read_tr16_b64_v4i16(lds<v4s>(a1));
  v2u b0 = *(v2u*)&r0, b1 = *(v2u*)&r1;
  return (v4u){b0[0], b0[1], b1[0], b1[1]};
}
__device__ __forceinline__ void lds_inc(u32 off) {
  asm volatile("s_waitcnt lgkmcnt(0)\n\ts_mov_b64 exec, 1\n\tds_add_u32 %0, %1\n\ts_mov_b64 exec, -1" :: "v"(off), "v"(1u) : "memory");
}
__device__ __forceinline__ v2u trh(u32 a) { v4s r = __builtin_amdgcn_ds_read_tr16_b64_v4i16(lds<v4s>(a)); return *(v2u*)&r; }
typedef u32 v16u __attribute__((ext_vector_type(16)));
typedef unsigned long long u64;
constexpr u32 OOB = 0x40000000u;
typedef unsigned short v2h __attribute__((ext_vector_type(2)));
__device__ __forceinline__ u32 pksub(u32 a, u32 b) { v2h r = __builtin_elementwise_sub_sat(*(v2h*)&a, *(v2h*)&b); return *(u32*)&r; }
constexpr v4f Z4 = {0.f, 0.f, 0.f, 0.f};

#define STAMP(k) do {} while (0)

__global__ void __launch_bounds__(512, 2)
mla_ffn_kernel(const u16*, const u16*, const u16*, const u16*, const u16*, const u16*, const u16*, const u16*, u16*, char*) {
  extern __shared__ __attribute__((aligned(16))) char smem_[];
  STAMP(0);
  v16u ka; v4u kb;
  asm volatile("s_load_dwordx16 %0, %2, 0x0\n\ts_load_dwordx4 %1, %2, 0x40" : "=&s"(ka), "=&s"(kb) : "s"(__builtin_amdgcn_kernarg_segment_ptr()));
  const u32 bi = blockIdx.x, xc = bi & 7, r = bi >> 3;
#define TB 0
  const u32 type = (r >> TB) & 1, pi = ((r >> (TB + 1)) << TB) | (r & ((1 << TB) - 1));
  const u32 chunk = xc * 16 + pi;
#define MAPT 1
#define MAPQ 1
  const u32 h = MAPT ? (1 - type) * 16 + pi : (1 - type) * 16 + xc * 2 + (pi >> 3), j = MAPT ? xc : pi & 7;
  const u32 hq = MAPQ ? pi + 16 * (xc & 1) : xc * 4 + (pi >> 2), p = MAPQ ? xc >> 1 : pi & 3;
  const u32 w = __builtin_amdgcn_readfirstlane(threadIdx.x >> 6), l = threadIdx.x & 63;
  const u32 l15 = l & 15, g = l >> 4, q4 = l15 >> 2, pp = l & 3;
  const v4u sv = {SENT, SENT, SENT, SENT};
  const u32 swz = (l15 ^ (2 * g + 4 * (w & 2))) * 16;
  u32 vq = (160 * p * 8192 + hq * 256) + (threadIdx.x >> 4) * 8192 + swz, dq = L_WQ + w * 1024;
  u32 l16 = l * 16, l4 = l * 4;
  asm volatile("" : "+v"(vq), "+v"(l16), "+v"(l4));
  asm volatile("s_waitcnt lgkmcnt(0)" : "+s"(ka), "+s"(kb), "+s"(dq));
#define KP(v, i) ((const char*)(((u64)v[2 * (i) + 1] << 32) | v[2 * (i)]))
  const char *x = KP(ka, 0), *ckv = KP(ka, 1), *wq = KP(ka, 2), *wuk = KP(ka, 3), *wuv = KP(ka, 4), *wo = KP(ka, 5), *w1 = KP(ka, 6), *w2 = KP(ka, 7);
  char *out = (char*)KP(kb, 0), *ws = (char*)KP(kb, 1);

  v4u xa; u32 xb;
  const bool lnw = w == 0;
  if (lnw) asm volatile("global_load_dwordx4 %0, %2, %4\n\tglobal_load_dword %1, %3, %4 offset:1024" : "=&v"(xa), "=&v"(xb) : "v"(l16), "v"(l4), "s"(x) : "memory");
  if (type == 0) dma_run<5, 32 * 8192, 8192>(wq, vq, dq);
  SCHED();
  const u32 trq = 2 * g * 1024 + q4 * 256 + (((2 * w + (pp >> 1)) ^ (2 * q4 + 8 * (g & 1))) * 16) + (pp & 1) * 8;
  const u32 vk = (128 * p + 32 * w + g) * 8192 + hq * 256 + (l15 ^ g) * 16, dk = L_WK + w * 8 * 1088;
  const u32 vkv = chunk * 131072 + (w & 3) * 4096 + l16, lkv = (w & 3) * 4 * RS;
  if (type) {
    if (w >= 4) kv_blk<W1, 0, 0, N0>(ckv, vkv, lkv);
    else {
      if (PFQ) dma_run<2 * PFQ, 16 * 8192, 0>(wq, vq + (5 - PFQ) * 32 * 8192, lkv + L_QA);
      if (PFC && w == 1 && pi < 15) {
        u64 pc;
        asm volatile("s_getpc_b64 %0" : "=s"(pc));
        dma((const char*)(pc & ~1023ull), pi * 1024 + l16, L_P);
      }
      if (PFK) { __builtin_amdgcn_s_sleep(DPK); dma_run<PFK, -4 * 8192, 0>(wuk, vk + 7 * 4 * 8192, lkv + L_QA); }
    }
  }
  if (w == 2 && l < 8) *lds<u32>(L_CNT + l4) = 0u;
  STAMP(27);
  u32 tag;
  asm volatile("s_load_dword %0, %1, 0x0" : "=s"(tag) : "s"(ws) : "memory");
  const rsrc_t rws = mk(ws, WS_END);
  if (type == 0 && w == 5) stc(sv, rws, l < 16 ? l16 : OOB, WS_QA + hq * 1024 + p * 256);
  if (WARM == 1 && type == 0 && w == 6) dma(ws, l < 32 ? WS_Q1 + hq * 2048 + p * 512 + l16 : WS_HINT + (l & 7) * 1024 + (hq * 4 + p) / 2 * 16, L_P);
  if (type) BAR();
  if (lnw) {
    if (2 * PFQ + PFK < 5 && type) VM0();
    asm volatile("s_waitcnt vmcnt(5)" : "+v"(xa), "+v"(xb) :: "memory");
    float f[10];
#pragma unroll
    for (int k = 0; k < 4; ++k) { const u32 t = xa[k]; f[2 * k] = lo2f(t); f[2 * k + 1] = hi2f(t); }
    f[8] = lo2f(xb); f[9] = hi2f(xb);
    float s = f[0];
#pragma unroll
    for (int k = 1; k < 10; ++k) s += f[k];
    const float mean = sum64(s) * (1.f / 640.f);
    float v = 0.f;
#pragma unroll
    for (int k = 0; k < 10; ++k) { const float a = f[k] - mean; v = fmaf(a, a, v); }
    const float mb = rbf(mean);
    u32 c[5];
#pragma unroll
    for (int k = 0; k < 5; ++k) c[k] = pk2(f[2 * k] - mb, f[2 * k + 1] - mb);
    v = rbf(sum64(v) * (1.f / 640.f));
    const float rs = __builtin_amdgcn_rcpf(rbf(__builtin_amdgcn_sqrtf(rbf(v + 1e-5f))));
#pragma unroll
    for (int k = 0; k < 5; ++k) c[k] = pk2(lo2f(c[k]) * rs, hi2f(c[k]) * rs);
    *lds<v4u>(L_X + l16) = (v4u){c[0], c[1], c[2], c[3]};
    *lds<u32>(L_X + 1024 + l4) = c[4];
    STAMP(14);
  }
  u32 vl = l15 * 128 + g * 32, sl = WS_Q1 + hq * 2048, lk = L_WK + w * 8 * 1088 + q4 * 1088 + pp * 256 + (g ^ pp) * 16;
  u32 vsa = (l & 17) == 0 ? l15 * 2 + (l & 32) : OOB, soa = WS_QA + hq * 1024 + p * 256 + w * 64;
  u32 vg = l * 8, sg = WS_HINT + xc * 1024 + type * 512;
  asm volatile("" : "+v"(vl), "+v"(lk), "+v"(vsa), "+v"(vg));
  asm volatile("" : "+s"(sl), "+s"(soa), "+s"(sg));
  if (type == 0) {
    BAR();
    STAMP(22);
    if (UKPOS == 0 && w < 4) dma_run<8, 4 * 8192, 1088>(wuk, vk, dk);
    asm volatile("" : "+s"(tag));
    v4u a[5], b[5];
#pragma unroll
    for (int s = 0; s < 5; ++s) b[s] = *lds<v4u>(L_X + p * 320 + s * 64 + g * 16);
    v4u qd = {0u, tag + 1u, 0u, tag + 1u};
    u32 ta = L_WQ + trq, vs = l15 == 0 ? g * 16 : OOB, so = WS_Q1 + hq * 2048 + p * 512 + w * 64;
    u32 vh = w == 5 && l < 8 ? l * 1024 : OOB, sh = WS_HINT + (hq * 4 + p) * 8;
    asm volatile("" : "+v"(qd), "+v"(ta), "+v"(vs), "+v"(vh), "+v"(b[0]), "+v"(b[1]), "+v"(b[2]), "+v"(b[3]), "+v"(b[4]));
    asm volatile("" : "+s"(so), "+s"(sh));
    if (WARM == 2) { stc((v4u){tag, tag, tag, tag}, rws, vs, so); stc2((v2u){tag, tag}, rws, vh, sh); }
    if (UKPOS == 0 && w < 4) asm volatile("s_waitcnt vmcnt(%0)" :: "n"(WARM == 2 ? 10 : 8) : "memory"); else asm volatile("s_waitcnt vmcnt(%0)" :: "n"(WARM == 2 ? 2 : 0) : "memory");
    STAMP(26);
    BAR();
    STAMP(1);
#pragma unroll
    for (int s = 0; s < 5; ++s) a[s] = trd(ta + s * 8192, ta + s * 8192 + 1024);
    SCHED();
    v4f d = Z4;
#pragma unroll
    for (int s = 0; s < 5; ++s) d = mfma(a[s], b[s], d);
    qd[0] = pk2(d[0], d[1]); qd[2] = pk2(d[2], d[3]);
    stc(qd, rws, vs, so);
    stc2((v2u){qd[1], (u32)__builtin_amdgcn_s_memrealtime()}, rws, vh, sh);
    if (w >= 4) lds_inc(L_CNT);
    STAMP(2);
  }
  asm volatile("" : "+s"(tag));
  tag += 1u;

  if (w < 4) {
    v2u hv;
    if (type == 0) {
      if (UKPOS == 1) dma_run<8, 4 * 8192, 1088>(wuk, vk, dk);
      __builtin_amdgcn_s_sleep(DL1);
      v4u pq[2];
      CLOB();
#pragma unroll
      for (int k = 0; k < 2; ++k) pq[k] = ldc(rws, vl + k * 16, sl);
      ldsst(L_CNT + 4, 1u);
      STAMP(23);
      asm volatile("s_waitcnt vmcnt(%0)" :: "n"(UKPOS ? 2 : WARM == 2 ? 6 : 4) : "memory");
      STAMP(24);
      v4u kf[2][4];
#pragma unroll
      for (int s = 0; s < 4; ++s)
#pragma unroll
        for (int T = 0; T < 2; ++T) kf[T][s] = *lds<v4u>(lk + T * 4 * 1088 + s * 64);
      lds_inc(L_CNT);
      float e[2];
      _Pragma("unroll 1") for (u32 it = 0; it < SPIN_CAP; ++it) {
        const v4u qf = {pq[0][0], pq[0][2], pq[1][0], pq[1][2]};
#pragma unroll
        for (int T = 0; T < 2; ++T) {
          const v4f d0 = mfma(qf, kf[T][0], Z4), d1 = mfma(qf, kf[T][1], Z4), d2 = mfma(qf, kf[T][2], Z4), d3 = mfma(qf, kf[T][3], Z4);
          e[T] = (d0[0] + d1[1]) + (d2[2] + d3[3]);
        }
        if (__all(((pq[0][1] ^ tag) | (pq[0][3] ^ tag) | (pq[1][1] ^ tag) | (pq[1][3] ^ tag)) == 0)) break;
        __builtin_amdgcn_s_sleep(SL1);
        CLOB();
#pragma unroll
        for (int k = 0; k < 2; ++k) pq[k] = ldc(rws, vl + k * 16, sl);
      }
      STAMP(3);
      float v = tr32(e[0], e[1]);
      v = tr16(v, v);
      const u32 pk = pk2(v, dpp<DPP_X1>(v));
      __builtin_amdgcn_raw_buffer_store_b32(pk, rws, vsa, soa, 16);
      STAMP(4);
    } else __builtin_amdgcn_s_sleep(DC1);
    if (w == 0) {
      v2u h1, h2;
      if (type) __builtin_amdgcn_s_sleep(DLG1);
      CLOB();
      hv = ldc2(rws, vg, sg);
      if (type) __builtin_amdgcn_s_sleep(SLG);
      CLOB();
      h1 = ldc2(rws, vg, sg);
      _Pragma("unroll 1") for (u32 it = 0; it < SPIN_CAP; ++it) {
        __builtin_amdgcn_s_sleep(SLG);
        CLOB();
        h2 = ldc2(rws, vg, sg);
        if (__all(hv[0] == tag)) break;
        __builtin_amdgcn_s_sleep(SLG);
        CLOB();
        hv = ldc2(rws, vg, sg);
        if (__all(h1[0] == tag)) { hv = h1; break; }
        __builtin_amdgcn_s_sleep(SLG);
        CLOB();
        h1 = ldc2(rws, vg, sg);
        if (__all(h2[0] == tag)) { hv = h2; break; }
      }
      STAMP(28);
      const u32 now = (u32)__builtin_amdgcn_s_memrealtime();
      float age = (float)(int)(now - hv[1]);
      age = -max64(-age);
      ldsst(L_CNT + 8, (now + DT - (u32)(int)fminf(fmaxf(age, 0.f), (float)DT)) | 1u);
    } else {
#pragma unroll 1
      for (u32 k = w - 1; k < 16; k += 3) stc(sv, rws, (l >> 3) * 16384 + (l & 7) * 16, WS_OP + (type * 16 + k) * 131072 + chunk * 128);
      if (w > 1) {
        stc(sv, rws, l16, WS_R + (1 - type) * R_SZ + chunk * 3584 + (w - 2) * 2048);
        stc(sv, rws, w == 2 || l < 32 ? l16 : OOB, WS_R + (1 - type) * R_SZ + chunk * 3584 + (w - 2) * 2048 + 1024);
      }
      STAMP(28);
    }
    v4u af[2][16], pa[4];
    u32 sqa = WS_QA + type * 16384 + w * 4096;
    u32 ab = L_KV + (16 * w + l15) * RS + g * 256, ab1 = ab + 64 * RS, bb = L_QA + l15 * RS + g * 256, iw = L_QA + 4 * w * RS + l16;
    u32 mw = g == 0 ? L_MX + l15 * 16 + w * 4 : L_MX + 256, pw = L_P + w * 1024 + l16;
    asm volatile("" : "+v"(ab), "+v"(ab1), "+v"(bb), "+v"(iw), "+v"(mw), "+v"(pw));
    asm volatile("" : "+s"(sqa));
    {
      u32 tg = 0;
      _Pragma("unroll 1") for (u32 it = 0; it < SPIN_CAP * 16; ++it) {
        tg = __builtin_amdgcn_readfirstlane(ldsld(L_CNT + 8));
        if (tg) break;
        __builtin_amdgcn_s_sleep(1);
      }
      _Pragma("unroll 1") for (u32 it = 0; it < 64; ++it)
        if ((int)((u32)__builtin_amdgcn_s_memrealtime() - tg) >= 0) break;
    }
    CLOB();
    STAMP(15);
#pragma unroll
    for (int k = 0; k < 4; ++k) pa[k] = ldc(rws, l16 + k * 1024, sqa);
    wait_kv(w + 1);
    STAMP(20);
#pragma unroll
    for (int k = 0; k < 16; ++k) af[0][k] = *lds<v4u>(ab + k * 16);
    _Pragma("unroll 1") for (u32 it = 0; it < SPIN_CAP; ++it) {
      u32 mm = 0;
#pragma unroll
      for (int k = 0; k < 4; ++k) {
        mm = max3u(max3u(mm, pa[k][0], pa[k][1]), pa[k][2], pa[k][3]);
        *lds<v4u>(iw + k * RS) = pa[k];
      }
      if (__all(mm != SENT)) break;
      __builtin_amdgcn_s_sleep(SL2);
      CLOB();
#pragma unroll
      for (int k = 0; k < 4; ++k) pa[k] = ldc(rws, l16 + k * 1024, sqa);
    }
    VM0();
    STAMP(5);
    BAR();
    STAMP(6);
    v4f s0 = Z4, s1 = Z4;
    {
      v4u bq[16];
#pragma unroll
      for (int s = 0; s < 16; ++s) bq[s] = *lds<v4u>(bb + s * 16);
      SCHED();
#pragma unroll
      for (int s = 0; s < 16; ++s) { af[1][s] = *lds<v4u>(ab1 + s * 16); s0 = mfma(af[0][s], bq[s], s0); SCHED(); }
#pragma unroll
      for (int s = 0; s < 16; ++s) s1 = mfma(af[1][s], bq[s], s1);
    }
    constexpr float SC = 0.08838834764831845f * 1.4426950408889634f;
    float mx = fmaxf(fmaxf(fmaxf(s0[0], s0[1]), fmaxf(s0[2], s0[3])), fmaxf(fmaxf(s1[0], s1[1]), fmaxf(s1[2], s1[3])));
    mx = __builtin_ceilf(mx32(mx16(mx)) * SC);
    *lds<float>(mw) = mx;
#define EX(v) __builtin_amdgcn_exp2f(fmaf(v, SC, -mx))
    *lds<v4u>(pw) = (v4u){pk2(EX(s0[0]), EX(s0[1])), pk2(EX(s0[2]), EX(s0[3])), pk2(EX(s1[0]), EX(s1[1])), pk2(EX(s1[2]), EX(s1[3]))};
    STAMP(17);
    BAR();
    STAMP(7);
    if (w == 0) {
      const v4f m4 = *lds<v4f>(L_MX + l15 * 16);
      const float M = fmaxf(fmaxf(m4[0], m4[1]), fmaxf(m4[2], m4[3]));
      float L = 0.f;
#pragma unroll
      for (int s = 0; s < 4; ++s) {
        const v4u pf = *lds<v4u>(L_P + s * 1024 + l16);
        float a = 0.f;
#pragma unroll
        for (int e = 0; e < 4; ++e) a = dot2(pf[e], 0x3F803F80u, a);
        L = fmaf(a, __builtin_amdgcn_exp2f(m4[s] - M), L);
      }
      L = tr16(L, L); L = tr32(L, L);
      if (g == 0) stc((v4u){f2u(M), tag, f2u(L), tag}, rws, l15 * 2048, WS_ML + type * 32768 + chunk * 16);
    }
    STAMP(8);
    __builtin_amdgcn_s_sleep(DLM);
  } else {
    const u32 v = w - 4, pg = L_CNT + 16 + 4 * v;
#define ROW0(b) ((b) < H0B ? (b) : (b) < H0B + AB ? (b) + 8 - AB - H0B : (b) - AB)
#define BLK0(b) do { if ((b) == 4) wait_ge(L_CNT, 8u); kv_blk<W0, b>(ckv, vkv + ROW0(b) * 16384, lkv); if ((b) + 1 > (W0 + 3) / 4) ldsst(pg, (b) + 1 - (W0 + 3) / 4); } while (0)
#define BLK1(b) do { kv_blk<W1, b>(ckv, vkv + (b) * 16384, lkv); if ((b) + 1 > (W1 + 3) / 4) ldsst(pg, (b) + 1 - (W1 + 3) / 4); } while (0)
    if (type == 0) {
      wait_ge(L_CNT + 4, 1u);
      STAMP(4);
      BLK0(0); BLK0(1); STAMP(23); BLK0(2); BLK0(3); STAMP(24); BLK0(4); BLK0(5); STAMP(25); BLK0(6); BLK0(7);
    } else {
      __builtin_amdgcn_s_sleep(DS1);
      STAMP(4);
      kv_blk<W1, 0, N0, 4>(ckv, vkv, lkv);
      BLK1(1); STAMP(23); BLK1(2); BLK1(3); STAMP(24); BLK1(4); BLK1(5); STAMP(25); BLK1(6); BLK1(7);
    }
    STAMP(3);
    v2u tl[4][8], th[4][8];
    u32 tb = L_KV + (4 * g + q4) * RS + v * 256 + (pp & 1) * 8 + (pp >> 1) * 128, tb1 = tb + 64 * RS;
    asm volatile("" : "+v"(tb), "+v"(tb1));
    wait_kv(4);
#pragma unroll
    for (int s = 0; s < 4; ++s)
#pragma unroll
      for (int t = 0; t < 8; ++t) tl[s][t] = trh(tb + 16 * s * RS + 16 * t);
    asm volatile("s_waitcnt vmcnt(8)" ::: "memory");
    ldsst(pg, 6u);
    wait_kv(6);
#pragma unroll
    for (int s = 0; s < 2; ++s)
#pragma unroll
      for (int t = 0; t < 8; ++t) th[s][t] = trh(tb1 + 16 * s * RS + 16 * t);
    u32 vu = (64 * j + 32 * (v & 1) + (v >> 1) + 8 * g) * 8192 + h * 256 + swz, v1 = (80 * j + 8 * v + (l >> 3)) * 3456 + h * 128 + (l & 7) * 16;
    u32 vo = g * 524288 + (l15 >> 3) * 16384 + (l & 7) * 16, so = WS_OP + (type * 128 + 2 * v) * 16384 + chunk * 128;
    u32 mr = L_MX + l15 * 16, pr = L_P + l16;
    asm volatile("" : "+v"(vu), "+v"(v1), "+v"(vo), "+v"(mr), "+v"(pr));
    asm volatile("" : "+s"(so));
    VM0();
    STAMP(5);
    BAR();
    STAMP(6);
    dma_run<4, 2 * 8192, 4096>(wuv, vu, L_UV + v * 1024);
    if (h < 27) {
      if (v < 2) dma_run<3, 32 * 3456, 4096>(w1, v1, L_W1 + v * 1024); else dma_run<2, 32 * 3456, 4096>(w1, v1, L_W1 + v * 1024);
    }
    __builtin_amdgcn_s_sleep(DPV);
#pragma unroll
    for (int s = 2; s < 4; ++s)
#pragma unroll
      for (int t = 0; t < 8; ++t) th[s][t] = trh(tb1 + 16 * s * RS + 16 * t);
    STAMP(17);
    BAR();
    STAMP(7);
    const v4f m4 = *lds<v4f>(mr);
    v4u pf[4];
#pragma unroll
    for (int s = 0; s < 4; ++s) pf[s] = *lds<v4u>(pr + s * 1024);
    const float M = fmaxf(fmaxf(m4[0], m4[1]), fmaxf(m4[2], m4[3]));
    v4f o[8];
#pragma unroll
    for (int s = 0; s < 4; ++s) {
      const u32 dl = (u32)fminf(M - m4[s], 255.f) * 0x800080u;
      const v4u ps = {pksub(pf[s][0], dl), pksub(pf[s][1], dl), pksub(pf[s][2], dl), pksub(pf[s][3], dl)};
#pragma unroll
      for (int t = 0; t < 8; ++t) o[t] = mfma(ps, (v4u){tl[s][t][0], tl[s][t][1], th[s][t][0], th[s][t][1]}, s ? o[t] : Z4);
    }
#pragma unroll
    for (int e = 0; e < 4; ++e)
      stc((v4u){pk2(o[0][e], o[1][e]), pk2(o[2][e], o[3][e]), pk2(o[4][e], o[5][e]), pk2(o[6][e], o[7][e])}, rws, vo, so + e * 131072);
    STAMP(8);
  }

  const u32 rcx = WS_R + type * R_SZ + (h & 15) * 8192;
  v4u a2[2], ow[4];
  auto late = [&]() {
    const rsrc_t ro = mk(wo, w < 5 ? 4096 * 640 * 2 : 0);
#pragma unroll
    for (int t = 0; t < 4; ++t) ow[t] = ldw(ro, (4 * g + t) * 1280 + l15 * 16, (h * 128 + j * 16) * 1280 + w * 256);
  };
  if (OW_EARLY) late();
  float rl;
  v4u uf[2];
  {
    v4u op[2], ml[2];
    _Pragma("unroll 1") for (u32 it = 0; it < SPIN_CAP; ++it) {
      CLOB();
#pragma unroll
      for (int t = 0; t < 2; ++t) ml[t] = ldc(rws, (l >> 3) * 16 + t * 128, WS_ML + h * 2048 + w * 256);
      if (__all(((ml[0][1] ^ tag) | (ml[0][3] ^ tag) | (ml[1][1] ^ tag) | (ml[1][3] ^ tag)) == 0)) break;
      __builtin_amdgcn_s_sleep(SL3);
    }
    STAMP(16);
    __builtin_amdgcn_s_sleep(DL3);
    CLOB();
#pragma unroll
    for (int t = 0; t < 2; ++t) op[t] = ldc(rws, l * 16 + t * 1024, WS_OP + (h * 8 + j) * 16384 + w * 2048);
    SCHED();
    const float m0 = u2f(ml[0][0]), m1 = u2f(ml[1][0]);
    float M = fmaxf(m0, m1);
    M = fmaxf(M, dpp<DPP_ROR8>(M)); M = mx32(mx16(M));
    float L = __builtin_amdgcn_exp2f(m0 - M) * u2f(ml[0][2]) + __builtin_amdgcn_exp2f(m1 - M) * u2f(ml[1][2]);
    L += dpp<DPP_ROR8>(L); L = tr16(L, L); L = tr32(L, L);
    if (l == 0) *lds<v2f>(L_ML + w * 8) = (v2f){M, L};
    BAR();
    STAMP(9);
    if (!OW_EARLY) late();
    const rsrc_t r2 = mk(w2, h < 27 && w < 5 ? 1728 * 640 * 2 : 0);
#pragma unroll
    for (int t = 0; t < 2; ++t) a2[t] = ldw(r2, (2 * g + t) * 1280 + l15 * 16, (64 * h + 8 * j) * 1280 + w * 256);
    SCHED();
    const v2f mi = *lds<v2f>(L_ML + (l & 7) * 8);
#pragma unroll
    for (int s = 0; s < 2; ++s) uf[s] = trd(L_UV + trq + s * 8192, L_UV + trq + s * 8192 + 1024);
    M = mi[0];
    M = fmaxf(M, dpp<DPP_X1>(M)); M = fmaxf(M, dpp<DPP_X2>(M)); M = fmaxf(M, dpp<DPP_HMIR>(M));
    L = __builtin_amdgcn_exp2f(mi[0] - M) * mi[1];
    L += dpp<DPP_X1>(L); L += dpp<DPP_X2>(L); L += dpp<DPP_HMIR>(L);
    rl = __builtin_amdgcn_rcpf(L);
    u32 cp = pk2(__builtin_amdgcn_exp2f(m0 - M), __builtin_amdgcn_exp2f(m1 - M));
    {
      v4f d = Z4;
      if (w < 4 && h < 27) {
        const u32 ta = L_W1 + (8 * g + q4) * 128 + w * 32 + pp * 8;
#pragma unroll
        for (int s = 0; s < 3; ++s) {
          v4u xf = *lds<v4u>(L_X + j * 160 + s * 64 + g * 16);
          const u32 tt = (s == 2 && g >= 2) ? ta + 48 * 128 : ta + s * 32 * 128;
          if (s == 2 && g >= 2) xf = (v4u){0u, 0u, 0u, 0u};
          d = mfma(xf, trd(tt, tt + 512), d);
        }
      }
      const float hp = d[0];
      __builtin_amdgcn_raw_buffer_store_b32(f2u(hp), rws, threadIdx.x < 256 && l < 16 ? (l >> 3) * 1024 + (l & 7) * 4 : 0x40000000u, rcx + w * 2048 + j * 128 + 64, 16);
    }
    asm volatile("" : "+v"(cp), "+v"(rl), "+v"(uf[0]), "+v"(uf[1]));
    STAMP(18);
    if (!__all(max3u(max4u(op[0]), max4u(op[1]), 0u) != SENT)) {
      _Pragma("unroll 1") for (u32 it = 0; it < SPIN_CAP; ++it) {
        __builtin_amdgcn_s_sleep(SL3);
        CLOB();
#pragma unroll
        for (int t = 0; t < 2; ++t) op[t] = ldc(rws, l * 16 + t * 1024, WS_OP + (h * 8 + j) * 16384 + w * 2048);
        if (__all(max3u(max4u(op[0]), max4u(op[1]), 0u) != SENT)) break;
      }
    }
    STAMP(10);
    float a[8];
#pragma unroll
    for (int e = 0; e < 4; ++e) {
      const u32 u0 = op[0][e], u1 = op[1][e];
      a[2 * e] = dot2(__builtin_amdgcn_perm(u1, u0, 0x05040100u), cp, 0.f);
      a[2 * e + 1] = dot2(__builtin_amdgcn_perm(u1, u0, 0x07060302u), cp, 0.f);
    }
    float o0, o1;
    red8(a, o0, o1);
    *lds<u32>(L_OA + (2 * w + ((l >> 3) & 1)) * 144 + ((l & 7) * 8 + g * 2) * 2) = pk2(o0, o1);
  }
  BAR();
  STAMP(19);
  {
    v4f c = mfma(*lds<v4u>(L_OA + l15 * 144 + g * 16), uf[0], Z4);
    c = mfma(*lds<v4u>(L_OA + l15 * 144 + 64 + g * 16), uf[1], c);
    float cs = (c[0] + c[1]) + (c[2] + c[3]);
    cs = tr16(cs, cs); cs = tr32(cs, cs);
    if (l < 16) __builtin_amdgcn_raw_buffer_store_b32(f2u(cs * rl), rws, l * 4, rcx + w * 1024 + j * 128, 16);
  }
  STAMP(11);
  if (w < 5) {
    __builtin_amdgcn_s_sleep(DL4);
    v4u cx; v2u hx;
    _Pragma("unroll 1") for (u32 it = 0; it < SPIN_CAP; ++it) {
      CLOB();
      cx = ldc(rws, (l & 7) * 128 + g * 16, rcx + j * 1024);
      hx = ldc2(rws, (l & 7) * 128 + g * 8 + 64, rcx + j * 1024);
      if (__all(max3u(max4u(cx), hx[0], hx[1]) != SENT)) break;
      __builtin_amdgcn_s_sleep(SL4);
    }
    STAMP(12);
    float a[8] = {0.f, 0.f, 0.f, 0.f, 0.f, 0.f, 0.f, 0.f};
#pragma unroll
    for (int t = 0; t < 4; ++t) {
      float c = u2f(cx[t]);
      c += dpp<DPP_X1>(c); c += dpp<DPP_X2>(c); c += dpp<DPP_HMIR>(c);
      fma8(a, ow[t], c);
    }
    float b[8] = {0.f, 0.f, 0.f, 0.f, 0.f, 0.f, 0.f, 0.f};
#pragma unroll
    for (int t = 0; t < 2; ++t) {
      float c = u2f(hx[t]);
      c += dpp<DPP_X1>(c); c += dpp<DPP_X2>(c); c += dpp<DPP_HMIR>(c);
      fma8(b, a2[t], fmaxf(c, 0.f));
    }
    float s0, s1, y0, y1;
    red8(a, s0, s1);
    red8(b, y0, y1);
    const u32 sp = pk2(s0, s1), yp = pk2(y0, y1);
    auto xs = __builtin_amdgcn_permlane16_swap(sp, sp, false, false);
    auto xy = __builtin_amdgcn_permlane16_swap(yp, yp, false, false);
    const u32 se = xs[0], so = xs[1], ye = xy[0], yo = xy[1];
    if ((g & 1) == 0) stc((v4u){se, ye, so, yo}, rws, l15 * 4096 + (g >> 1) * 16, WS_R + type * R_SZ + R_F + w * 16 * 4096 + chunk * 32);
    ldsst(L_CNT + 12, 1u);
  } else if (w == 5 && bi < 80) {
    const u32 me = 8 * bi + 4 * (l >> 5) + (l & 1) + ((l >> 1) & 2);
    const u32 xv = ((const u16*)x)[me];
    const u32 one = (l15 < 8) ? (0x3F80u << (16 * (l & 1))) : 0u;
    const v4u sel = {(l15 >> 1) == 0 ? one : 0u, (l15 >> 1) == 1 ? one : 0u, (l15 >> 1) == 2 ? one : 0u, (l15 >> 1) == 3 ? one : 0u};
    _Pragma("unroll 1") for (u32 it = 0; it < SPIN_CAP; ++it) {
      CLOB();
      const v2u t0 = ldc2(rws, l * 16, WS_ML + (h ^ 16) * 2048), t1 = ldc2(rws, l * 16 + 1024, WS_ML + (h ^ 16) * 2048);
      if (__all(t0[1] == tag && t1[1] == tag)) break;
      __builtin_amdgcn_s_sleep(SL5);
    }
    wait_ge(L_CNT + 12, 1u);
    __builtin_amdgcn_s_sleep(DL5);
    v4u f[8];
    _Pragma("unroll 1") for (u32 it = 0; it < SPIN_CAP; ++it) {
      CLOB();
      u32 mm = 0;
#pragma unroll
      for (int t = 0; t < 8; ++t) f[t] = ldc(rws, l * 16 + (t & 3) * 1024, WS_R + (t >> 2) * R_SZ + R_F + bi * 4096);
#pragma unroll
      for (int t = 0; t < 8; ++t) mm = max3u(max3u(mm, f[t][0], f[t][1]), f[t][2], f[t][3]);
      if (__all(mm != SENT)) break;
      __builtin_amdgcn_s_sleep(SL5);
    }
    STAMP(12);
    v4f d0 = Z4, d1 = Z4;
#pragma unroll
    for (int t = 0; t < 4; ++t) { d0 = mfma(f[2 * t], sel, d0); d1 = mfma(f[2 * t + 1], sel, d1); }
    d0 += d1;
    float v = tr32(d0[0] + d0[2], d0[1] + d0[3]);
    v = rbf(tr16(v, v));
    const float yv = dpp<0xEE>(v);
    const float gt = rbf(yv * __builtin_amdgcn_rcpf(1.f + __builtin_amdgcn_exp2f(-1.4426950408889634f * yv)));
    const u32 ob = pk2(lo2f(xv) + rbf(v * gt), 0.f);
    if ((l & 0x1A) == 0) ((u16*)out)[me] = (u16)ob;
    if (bi == 0 && l == 0) __builtin_amdgcn_raw_buffer_store_b32(tag, rws, 0, WS_EPOCH, 16);
  }
}

extern "C" void kernel_launch(void* const* d_in, const int* in_sizes, int n_in, void* d_out, int out_size, void* d_ws, size_t ws_size,
                              hipStream_t stream) {
  (void)in_sizes; (void)n_in; (void)out_size; (void)ws_size;
  (void)hipFuncSetAttribute((const void*)mla_ffn_kernel, hipFuncAttributeMaxDynamicSharedMemorySize, L_TOTAL);
  mla_ffn_kernel<<<256, 512, L_TOTAL, stream>>>((const u16*)d_in[0], (const u16*)d_in[1], (const u16*)d_in[2], (const u16*)d_in[3],
                                                (const u16*)d_in[4], (const u16*)d_in[5], (const u16*)d_in[7], (const u16*)d_in[8],
                                                (u16*)d_out, (char*)d_ws);
}
```

```cpp
#define STAMPS
#include <hip/hip_runtime.h>
#include <hip/hip_bf16.h>
#include <stdint.h>

typedef unsigned short u16;
typedef unsigned int u32;
typedef short v4s __attribute__((ext_vector_type(4)));
typedef __bf16 v8bf __attribute__((ext_vector_type(8)));
typedef __bf16 v2bf __attribute__((ext_vector_type(2)));
typedef float v4f __attribute__((ext_vector_type(4)));
typedef float v2f __attribute__((ext_vector_type(2)));
typedef u32 v4u __attribute__((ext_vector_type(4)));
typedef u32 v2u __attribute__((ext_vector_type(2)));
#define LAS __attribute__((address_space(3)))
typedef __amdgpu_buffer_rsrc_t rsrc_t;

#define W_AUX 0
#define KV_NT ""
#define N0 0
#define DS1 10
#define W1 5
#define W0 8
#define H0B 2
#define AB 0
#define DC1 60
#define DT 150
#define DPV 4
#define PFQ 0
#define PFK 0
#define UKPOS 0
#define DPK 0
#define WARM 0
#define PFC 1
#define OW_EARLY 0
#define DL1 8
#define SL1 1
#define DLG1 1
#define SLG 6
#define SL2 4
#define DLM 10
#define SL3 2
#define DL3 0
#define DL4 6
#define SL4 1
#define SL5 2
#define DL5 10

constexpr u32 WS_EPOCH = 0;
constexpr u32 WS_HINT = 4096;
constexpr u32 WS_Q1 = WS_HINT + 8192;
constexpr u32 WS_QA = WS_Q1 + 65536;
constexpr u32 WS_ML = WS_QA + 32768;
constexpr u32 WS_R = WS_ML + 65536;
constexpr u32 R_F = 131072, R_SZ = R_F + 80 * 4096;
constexpr u32 WS_OP = WS_R + 2 * R_SZ;
constexpr u32 WS_END = WS_OP + 256 * 16384;
constexpr u32 WS_STAMP = WS_END;
static_assert(R_SZ == 128 * 3584, "clear split");

constexpr u32 RS = 1040;
constexpr u32 L_KV = 0;
constexpr u32 L_WK = 64 * RS;
constexpr u32 L_WQ = L_WK + 32 * 1088;
constexpr u32 L_UV = 0, L_W1 = 16384;
constexpr u32 L_QA = 128 * RS;
constexpr u32 L_P = L_QA + 16 * RS;
constexpr u32 L_X = L_P + 4096;
constexpr u32 L_MX = L_X + 1344;
constexpr u32 L_OA = L_MX + 272;
constexpr u32 L_ML = L_OA + 16 * 144;
constexpr u32 L_CNT = L_ML + 64;
constexpr u32 L_TOTAL = L_CNT + 64;
static_assert(L_WQ + 40960 <= L_P && L_W1 + 10240 <= 32 * RS, "staging");
static_assert(L_TOTAL <= 163840, "lds");

constexpr u32 SENT = 0xFFFFFFFFu;
constexpr u32 SPIN_CAP = 1u << 18;

__device__ __forceinline__ float u2f(u32 u) { return __uint_as_float(u); }
__device__ __forceinline__ u32 f2u(float f) { return __float_as_uint(f); }
__device__ __forceinline__ float lo2f(u32 u) { return __uint_as_float(u << 16); }
__device__ __forceinline__ float hi2f(u32 u) { return __uint_as_float(u & 0xFFFF0000u); }
__device__ __forceinline__ u32 pk2(float a, float b) { v2f f = {a, b}; v2bf c = __builtin_convertvector(f, v2bf); return *(u32*)&c; }
__device__ __forceinline__ float rbf(float a) { return lo2f(pk2(a, a)); }
__device__ __forceinline__ u32 canon(u32 u) { return u < 0xFFFFFFFEu ? u : 0xFFFFFFFEu; }
__device__ __forceinline__ u32 max3u(u32 a, u32 b, u32 c) { u32 m = a > b ? a : b; return m > c ? m : c; }
__device__ __forceinline__ u32 max4u(v4u v) { return max3u(max3u(v[0], v[1], v[2]), v[3], v[3]); }
template <int CTRL> __device__ __forceinline__ float dpp(float v) { return u2f(__builtin_amdgcn_mov_dpp(f2u(v), CTRL, 0xF, 0xF, true)); }
constexpr int DPP_X1 = 0xB1, DPP_X2 = 0x4E, DPP_ROR4 = 0x124, DPP_ROR8 = 0x128, DPP_HMIR = 0x141;
__device__ __forceinline__ float sum16(float v) { v += dpp<DPP_X1>(v); v += dpp<DPP_X2>(v); v += dpp<DPP_ROR4>(v); v += dpp<DPP_ROR8>(v); return v; }
__device__ __forceinline__ float max16(float v) { v = fmaxf(v, dpp<DPP_X1>(v)); v = fmaxf(v, dpp<DPP_X2>(v)); v = fmaxf(v, dpp<DPP_ROR4>(v)); v = fmaxf(v, dpp<DPP_ROR8>(v)); return v; }
__device__ __forceinline__ float tr16(float a, float b) { auto r = __builtin_amdgcn_permlane16_swap(f2u(a), f2u(b), false, false); u32 x = r[0], y = r[1]; return u2f(x) + u2f(y); }
__device__ __forceinline__ float tr32(float a, float b) { auto r = __builtin_amdgcn_permlane32_swap(f2u(a), f2u(b), false, false); u32 x = r[0], y = r[1]; return u2f(x) + u2f(y); }
__device__ __forceinline__ float mx16(float a) { auto r = __builtin_amdgcn_permlane16_swap(f2u(a), f2u(a), false, false); u32 x = r[0], y = r[1]; return fmaxf(u2f(x), u2f(y)); }
__device__ __forceinline__ float mx32(float a) { auto r = __builtin_amdgcn_permlane32_swap(f2u(a), f2u(a), false, false); u32 x = r[0], y = r[1]; return fmaxf(u2f(x), u2f(y)); }
__device__ __forceinline__ float sum64(float v) { v = sum16(v); v = tr16(v, v); return tr32(v, v); }
__device__ __forceinline__ float max64(float v) { v = max16(v); v = mx16(v); return mx32(v); }
__device__ __forceinline__ float dot2(u32 a, u32 b, float c) { return __builtin_amdgcn_fdot2_f32_bf16(*(v2bf*)&a, *(v2bf*)&b, c, false); }
__device__ __forceinline__ v4f mfma(v4u a, v4u b, v4f c) { return __builtin_amdgcn_mfma_f32_16x16x32_bf16(*(v8bf*)&a, *(v8bf*)&b, c, 0, 0, 0); }
__device__ __forceinline__ rsrc_t mk(const void* p, u32 n) { return __builtin_amdgcn_make_buffer_rsrc((void*)p, 0, n, 0x00020000); }
__device__ __forceinline__ v4u ldw(rsrc_t r, u32 v, u32 s) { return __builtin_amdgcn_raw_buffer_load_b128(r, v, s, W_AUX); }
__device__ __forceinline__ v4u ldc(rsrc_t r, u32 v, u32 s) { return __builtin_amdgcn_raw_buffer_load_b128(r, v, s, 16); }
__device__ __forceinline__ v2u ldc2(rsrc_t r, u32 v, u32 s) { return __builtin_amdgcn_raw_buffer_load_b64(r, v, s, 16); }
__device__ __forceinline__ void stc(v4u d, rsrc_t r, u32 v, u32 s) {
  __builtin_amdgcn_raw_buffer_store_b128(d, r, v, s, 16);
  asm volatile("s_nop 1" :: "v"(d));
}
__device__ __forceinline__ void stc2(v2u d, rsrc_t r, u32 v, u32 s) { __builtin_amdgcn_raw_buffer_store_b64(d, r, v, s, 16); }
__device__ __forceinline__ u16 ldx(rsrc_t r, u32 elem) { return __builtin_amdgcn_raw_buffer_load_b16(r, elem * 2, 0, 0); }
#define BAR() asm volatile("s_waitcnt lgkmcnt(0)\n\ts_barrier" ::: "memory")
#define SCHED() __builtin_amdgcn_sched_barrier(0)
#define CLOB() asm volatile("" ::: "memory")
#define CVM0() __builtin_amdgcn_s_waitcnt(0x0F70)
#define VM0() asm volatile("s_waitcnt vmcnt(0)" ::: "memory")
template <typename T> __device__ __forceinline__ LAS T* lds(u32 off) { return (LAS T*)(uintptr_t)off; }
__device__ __forceinline__ void fma8(float* a, v4u w, float x) {
#pragma unroll
  for (int e = 0; e < 4; ++e) { a[2 * e] = fmaf(lo2f(w[e]), x, a[2 * e]); a[2 * e + 1] = fmaf(hi2f(w[e]), x, a[2 * e + 1]); }
}
__device__ __forceinline__ void red8(const float* a, float& o0, float& o1) {
  float t0 = tr32(a[0], a[4]), t1 = tr32(a[1], a[5]), t2 = tr32(a[2], a[6]), t3 = tr32(a[3], a[7]);
  o0 = tr16(t0, t2); o1 = tr16(t1, t3);
}

__device__ __forceinline__ u32 ldsld(u32 off) { return __hip_atomic_load(lds<u32>(off), __ATOMIC_RELAXED, __HIP_MEMORY_SCOPE_WORKGROUP); }
__device__ __forceinline__ void ldsst(u32 off, u32 v) { __hip_atomic_store(lds<u32>(off), v, __ATOMIC_RELAXED, __HIP_MEMORY_SCOPE_WORKGROUP); }
__device__ __forceinline__ void wait_ge(u32 off, u32 v) {
  _Pragma("unroll 1") for (u32 it = 0; it < SPIN_CAP * 16; ++it) {
    if ((u32)__builtin_amdgcn_readfirstlane(ldsld(off)) >= v) break;
  }
  CLOB();
}
__device__ __forceinline__ void wait_kv(u32 v) {
  _Pragma("unroll 1") for (u32 it = 0; it < SPIN_CAP * 16; ++it) {
    CLOB();
    const v4u a = *(volatile LAS v4u*)lds<v4u>(L_CNT + 16);
    const u32 m = min(min(a[0], a[1]), min(a[2], a[3]));
    if ((u32)__builtin_amdgcn_readfirstlane(m) >= v) break;
  }
  CLOB();
}
template <int W, int SLOT> __device__ __forceinline__ void kv_row(const char* src, u32 voff, u32 lb) {
  asm volatile("s_add_u32 m0, %2, %3\n\ts_waitcnt vmcnt(%4)\n\tglobal_load_lds_dwordx4 %0, %1 offset:%5" KV_NT
               :: "v"(voff), "s"(src), "s"(lb), "n"(SLOT * RS - (SLOT & 3) * 1024), "n"(W - 1), "n"((SLOT & 3) * 1024) : "memory", "scc");
}
template <int W, int B, int FROM = 0, int TO = 4> __device__ __forceinline__ void kv_blk(const char* src, u32 voff, u32 lb) {
  if (FROM <= 0 && TO > 0) kv_row<W, 16 * B>(src, voff, lb);
  if (FROM <= 1 && TO > 1) kv_row<W, 16 * B + 1>(src, voff, lb);
  if (FROM <= 2 && TO > 2) kv_row<W, 16 * B + 2>(src, voff, lb);
  if (FROM <= 3 && TO > 3) kv_row<W, 16 * B + 3>(src, voff, lb);
}
template <int N, int GS, int LS> __device__ __forceinline__ void dma_run(const char* src, u32 voff, u32 dst) {
  asm volatile("s_mov_b32 m0, %2\n\ts_nop 0\n\tglobal_load_lds_dwordx4 %0, %1\n\t"
               ".rept %3\n\ts_add_u32 m0, m0, %5\n\tv_add_u32 %0, %4, %0\n\tglobal_load_lds_dwordx4 %0, %1\n\t.endr"
               : "+v"(voff) : "s"(src), "s"(dst), "n"(N - 1), "n"(GS), "n"(LS) : "memory", "scc");
}
__device__ __forceinline__ void dma(const char* src, u32 voff, u32 dst) {
  asm volatile("s_mov_b32 m0, %2\n\ts_nop 0\n\tglobal_load_lds_dwordx4 %0, %1" :: "v"(voff), "s"(src), "s"(dst) : "memory");
}
__device__ __forceinline__ v4u trd(u32 a0, u32 a1) {
  v4s r0 = __builtin_amdgcn_ds_read_tr16_b64_v4i16(lds<v4s>(a0)), r1 = __builtin_amdgcn_ds_read_tr16_b64_v4i16(lds<v4s>(a1));
  v2u b0 = *(v2u*)&r0, b1 = *(v2u*)&r1;
  return (v4u){b0[0], b0[1], b1[0], b1[1]};
}
__device__ __forceinline__ void lds_inc(u32 off) {
  asm volatile("s_waitcnt lgkmcnt(0)\n\ts_mov_b64 exec, 1\n\tds_add_u32 %0, %1\n\ts_mov_b64 exec, -1" :: "v"(off), "v"(1u) : "memory");
}
__device__ __forceinline__ v2u trh(u32 a) { v4s r = __builtin_amdgcn_ds_read_tr16_b64_v4i16(lds<v4s>(a)); return *(v2u*)&r; }
typedef u32 v16u __attribute__((ext_vector_type(16)));
typedef unsigned long long u64;
constexpr u32 OOB = 0x40000000u;
typedef unsigned short v2h __attribute__((ext_vector_type(2)));
__device__ __forceinline__ u32 pksub(u32 a, u32 b) { v2h r = __builtin_elementwise_sub_sat(*(v2h*)&a, *(v2h*)&b); return *(u32*)&r; }
constexpr v4f Z4 = {0.f, 0.f, 0.f, 0.f};

#define NST 32
#define STAMP(k) do { st[k] = (u32)__builtin_amdgcn_s_memrealtime(); } while (0)

__global__ void __launch_bounds__(512, 2)
mla_ffn_kernel(const u16*, const u16*, const u16*, const u16*, const u16*, const u16*, const u16*, const u16*, u16*, char*) {
  extern __shared__ __attribute__((aligned(16))) char smem_[];
  u32 st[NST];
#pragma unroll
  for (int k = 0; k < NST; ++k) st[k] = 0;
  STAMP(0);
  const unsigned long long clk0 = __builtin_amdgcn_s_memtime();
  v16u ka; v4u kb;
  asm volatile("s_load_dwordx16 %0, %2, 0x0\n\ts_load_dwordx4 %1, %2, 0x40" : "=&s"(ka), "=&s"(kb) : "s"(__builtin_amdgcn_kernarg_segment_ptr()));
  const u32 bi = blockIdx.x, xc = bi & 7, r = bi >> 3;
#define TB 0
  const u32 type = (r >> TB) & 1, pi = ((r >> (TB + 1)) << TB) | (r & ((1 << TB) - 1));
  const u32 chunk = xc * 16 + pi;
#define MAPT 1
#define MAPQ 1
  const u32 h = MAPT ? (1 - type) * 16 + pi : (1 - type) * 16 + xc * 2 + (pi >> 3), j = MAPT ? xc : pi & 7;
  const u32 hq = MAPQ ? pi + 16 * (xc & 1) : xc * 4 + (pi >> 2), p = MAPQ ? xc >> 1 : pi & 3;
  const u32 w = __builtin_amdgcn_readfirstlane(threadIdx.x >> 6), l = threadIdx.x & 63;
  const u32 l15 = l & 15, g = l >> 4, q4 = l15 >> 2, pp = l & 3;
  const v4u sv = {SENT, SENT, SENT, SENT};
  const u32 swz = (l15 ^ (2 * g + 4 * (w & 2))) * 16;
  u32 vq = (160 * p * 8192 + hq * 256) + (threadIdx.x >> 4) * 8192 + swz, dq = L_WQ + w * 1024;
  u32 l16 = l * 16, l4 = l * 4;
  asm volatile("" : "+v"(vq), "+v"(l16), "+v"(l4));
  unsigned long long tB, tC;
  asm volatile("s_memrealtime %0" : "=s"(tB));
  asm volatile("s_waitcnt lgkmcnt(0)" : "+s"(ka), "+s"(kb), "+s"(dq));
  asm volatile("s_memrealtime %0" : "=s"(tC));
#define KP(v, i) ((const char*)(((u64)v[2 * (i) + 1] << 32) | v[2 * (i)]))
  const char *x = KP(ka, 0), *ckv = KP(ka, 1), *wq = KP(ka, 2), *wuk = KP(ka, 3), *wuv = KP(ka, 4), *wo = KP(ka, 5), *w1 = KP(ka, 6), *w2 = KP(ka, 7);
  char *out = (char*)KP(kb, 0), *ws = (char*)KP(kb, 1);

  v4u xa; u32 xb;
  const bool lnw = w == 0;
  if (lnw) asm volatile("global_load_dwordx4 %0, %2, %4\n\tglobal_load_dword %1, %3, %4 offset:1024" : "=&v"(xa), "=&v"(xb) : "v"(l16), "v"(l4), "s"(x) : "memory");
  if (type == 0) dma_run<5, 32 * 8192, 8192>(wq, vq, dq);
  SCHED();
  const u32 trq = 2 * g * 1024 + q4 * 256 + (((2 * w + (pp >> 1)) ^ (2 * q4 + 8 * (g & 1))) * 16) + (pp & 1) * 8;
  const u32 vk = (128 * p + 32 * w + g) * 8192 + hq * 256 + (l15 ^ g) * 16, dk = L_WK + w * 8 * 1088;
  const u32 vkv = chunk * 131072 + (w & 3) * 4096 + l16, lkv = (w & 3) * 4 * RS;
  if (type) {
    if (w >= 4) kv_blk<W1, 0, 0, N0>(ckv, vkv, lkv);
    else {
      if (PFQ) dma_run<2 * PFQ, 16 * 8192, 0>(wq, vq + (5 - PFQ) * 32 * 8192, lkv + L_QA);
      if (PFC && w == 1 && pi < 15) {
        u64 pc;
        asm volatile("s_getpc_b64 %0" : "=s"(pc));
        dma((const char*)(pc & ~1023ull), pi * 1024 + l16, L_P);
      }
      if (PFK) { __builtin_amdgcn_s_sleep(DPK); dma_run<PFK, -4 * 8192, 0>(wuk, vk + 7 * 4 * 8192, lkv + L_QA); }
    }
  }
  if (w == 2 && l < 8) *lds<u32>(L_CNT + l4) = 0u;
  STAMP(27);
  u32 tag;
  asm volatile("s_load_dword %0, %1, 0x0" : "=s"(tag) : "s"(ws) : "memory");
  const rsrc_t rws = mk(ws, WS_END);
  if (type == 0 && w == 5) stc(sv, rws, l < 16 ? l16 : OOB, WS_QA + hq * 1024 + p * 256);
  if (WARM == 1 && type == 0 && w == 6) dma(ws, l < 32 ? WS_Q1 + hq * 2048 + p * 512 + l16 : WS_HINT + (l & 7) * 1024 + (hq * 4 + p) / 2 * 16, L_P);
  if (type) BAR();
  if (lnw) {
    if (2 * PFQ + PFK < 5 && type) VM0();
    asm volatile("s_waitcnt vmcnt(5)" : "+v"(xa), "+v"(xb) :: "memory");
    float f[10];
#pragma unroll
    for (int k = 0; k < 4; ++k) { const u32 t = xa[k]; f[2 * k] = lo2f(t); f[2 * k + 1] = hi2f(t); }
    f[8] = lo2f(xb); f[9] = hi2f(xb);
    float s = f[0];
#pragma unroll
    for (int k = 1; k < 10; ++k) s += f[k];
    const float mean = sum64(s) * (1.f / 640.f);
    float v = 0.f;
#pragma unroll
    for (int k = 0; k < 10; ++k) { const float a = f[k] - mean; v = fmaf(a, a, v); }
    const float mb = rbf(mean);
    u32 c[5];
#pragma unroll
    for (int k = 0; k < 5; ++k) c[k] = pk2(f[2 * k] - mb, f[2 * k + 1] - mb);
    v = rbf(sum64(v) * (1.f / 640.f));
    const float rs = __builtin_amdgcn_rcpf(rbf(__builtin_amdgcn_sqrtf(rbf(v + 1e-5f))));
#pragma unroll
    for (int k = 0; k < 5; ++k) c[k] = pk2(lo2f(c[k]) * rs, hi2f(c[k]) * rs);
    *lds<v4u>(L_X + l16) = (v4u){c[0], c[1], c[2], c[3]};
    *lds<u32>(L_X + 1024 + l4) = c[4];
    STAMP(14);
  }
  u32 vl = l15 * 128 + g * 32, sl = WS_Q1 + hq * 2048, lk = L_WK + w * 8 * 1088 + q4 * 1088 + pp * 256 + (g ^ pp) * 16;
  u32 vsa = (l & 17) == 0 ? l15 * 2 + (l & 32) : OOB, soa = WS_QA + hq * 1024 + p * 256 + w * 64;
  u32 vg = l * 8, sg = WS_HINT + xc * 1024 + type * 512;
  asm volatile("" : "+v"(vl), "+v"(lk), "+v"(vsa), "+v"(vg));
  asm volatile("" : "+s"(sl), "+s"(soa), "+s"(sg));
  if (type == 0) {
    BAR();
    STAMP(22);
    if (UKPOS == 0 && w < 4) dma_run<8, 4 * 8192, 1088>(wuk, vk, dk);
    asm volatile("" : "+s"(tag));
    v4u a[5], b[5];
#pragma unroll
    for (int s = 0; s < 5; ++s) b[s] = *lds<v4u>(L_X + p * 320 + s * 64 + g * 16);
    v4u qd = {0u, tag + 1u, 0u, tag + 1u};
    u32 ta = L_WQ + trq, vs = l15 == 0 ? g * 16 : OOB, so = WS_Q1 + hq * 2048 + p * 512 + w * 64;
    u32 vh = w == 5 && l < 8 ? l * 1024 : OOB, sh = WS_HINT + (hq * 4 + p) * 8;
    asm volatile("" : "+v"(qd), "+v"(ta), "+v"(vs), "+v"(vh), "+v"(b[0]), "+v"(b[1]), "+v"(b[2]), "+v"(b[3]), "+v"(b[4]));
    asm volatile("" : "+s"(so), "+s"(sh));
    if (WARM == 2) { stc((v4u){tag, tag, tag, tag}, rws, vs, so); stc2((v2u){tag, tag}, rws, vh, sh); }
    if (UKPOS == 0 && w < 4) asm volatile("s_waitcnt vmcnt(%0)" :: "n"(WARM == 2 ? 10 : 8) : "memory"); else asm volatile("s_waitcnt vmcnt(%0)" :: "n"(WARM == 2 ? 2 : 0) : "memory");
    STAMP(26);
    BAR();
    STAMP(1);
#pragma unroll
    for (int s = 0; s < 5; ++s) a[s] = trd(ta + s * 8192, ta + s * 8192 + 1024);
    SCHED();
    v4f d = Z4;
#pragma unroll
    for (int s = 0; s < 5; ++s) d = mfma(a[s], b[s], d);
    qd[0] = pk2(d[0], d[1]); qd[2] = pk2(d[2], d[3]);
    stc(qd, rws, vs, so);
    stc2((v2u){qd[1], (u32)__builtin_amdgcn_s_memrealtime()}, rws, vh, sh);
    if (w >= 4) lds_inc(L_CNT);
    STAMP(2);
  }
  asm volatile("" : "+s"(tag));
  tag += 1u;
  asm volatile("" : "+s"(tB), "+s"(tC)); st[29] = (u32)tB; st[13] = (u32)tC;

  if (w < 4) {
    v2u hv;
    if (type == 0) {
      if (UKPOS == 1) dma_run<8, 4 * 8192, 1088>(wuk, vk, dk);
      __builtin_amdgcn_s_sleep(DL1);
      v4u pq[2];
      CLOB();
#pragma unroll
      for (int k = 0; k < 2; ++k) pq[k] = ldc(rws, vl + k * 16, sl);
      ldsst(L_CNT + 4, 1u);
      STAMP(23);
      asm volatile("s_waitcnt vmcnt(%0)" :: "n"(UKPOS ? 2 : WARM == 2 ? 6 : 4) : "memory");
      STAMP(24);
      v4u kf[2][4];
#pragma unroll
      for (int s = 0; s < 4; ++s)
#pragma unroll
        for (int T = 0; T < 2; ++T) kf[T][s] = *lds<v4u>(lk + T * 4 * 1088 + s * 64);
      lds_inc(L_CNT);
      float e[2];
      VM0(); STAMP(25);
      _Pragma("unroll 1") for (u32 it = 0; it < SPIN_CAP; ++it) {
        const v4u qf = {pq[0][0], pq[0][2], pq[1][0], pq[1][2]};
#pragma unroll
        for (int T = 0; T < 2; ++T) {
          const v4f d0 = mfma(qf, kf[T][0], Z4), d1 = mfma(qf, kf[T][1], Z4), d2 = mfma(qf, kf[T][2], Z4), d3 = mfma(qf, kf[T][3], Z4);
          e[T] = (d0[0] + d1[1]) + (d2[2] + d3[3]);
        }
        if (__all(((pq[0][1] ^ tag) | (pq[0][3] ^ tag) | (pq[1][1] ^ tag) | (pq[1][3] ^ tag)) == 0)) break;
        __builtin_amdgcn_s_sleep(SL1);
        CLOB();
#pragma unroll
        for (int k = 0; k < 2; ++k) pq[k] = ldc(rws, vl + k * 16, sl);
        st[21] += 1u << 24;
      }
      STAMP(3);
      float v = tr32(e[0], e[1]);
      v = tr16(v, v);
      const u32 pk = pk2(v, dpp<DPP_X1>(v));
      __builtin_amdgcn_raw_buffer_store_b32(pk, rws, vsa, soa, 16);
      STAMP(4);
    } else __builtin_amdgcn_s_sleep(DC1);
    if (w == 0) {
      v2u h1, h2;
      if (type) __builtin_amdgcn_s_sleep(DLG1);
      CLOB();
      hv = ldc2(rws, vg, sg);
      if (type) __builtin_amdgcn_s_sleep(SLG);
      CLOB();
      h1 = ldc2(rws, vg, sg);
      _Pragma("unroll 1") for (u32 it = 0; it < SPIN_CAP; ++it) {
        __builtin_amdgcn_s_sleep(SLG);
        CLOB();
        h2 = ldc2(rws, vg, sg);
        if (__all(hv[0] == tag)) break;
        __builtin_amdgcn_s_sleep(SLG);
        CLOB();
        hv = ldc2(rws, vg, sg);
        if (__all(h1[0] == tag)) { hv = h1; break; }
        __builtin_amdgcn_s_sleep(SLG);
        CLOB();
        h1 = ldc2(rws, vg, sg);
        if (__all(h2[0] == tag)) { hv = h2; break; }
      }
      STAMP(28);
      const u32 now = (u32)__builtin_amdgcn_s_memrealtime();
      float age = (float)(int)(now - hv[1]);
      age = -max64(-age);
      ldsst(L_CNT + 8, (now + DT - (u32)(int)fminf(fmaxf(age, 0.f), (float)DT)) | 1u);
    } else {
#pragma unroll 1
      for (u32 k = w - 1; k < 16; k += 3) stc(sv, rws, (l >> 3) * 16384 + (l & 7) * 16, WS_OP + (type * 16 + k) * 131072 + chunk * 128);
      if (w > 1) {
        stc(sv, rws, l16, WS_R + (1 - type) * R_SZ + chunk * 3584 + (w - 2) * 2048);
        stc(sv, rws, w == 2 || l < 32 ? l16 : OOB, WS_R + (1 - type) * R_SZ + chunk * 3584 + (w - 2) * 2048 + 1024);
      }
      STAMP(28);
    }
    v4u af[2][16], pa[4];
    u32 sqa = WS_QA + type * 16384 + w * 4096;
    u32 ab = L_KV + (16 * w + l15) * RS + g * 256, ab1 = ab + 64 * RS, bb = L_QA + l15 * RS + g * 256, iw = L_QA + 4 * w * RS + l16;
    u32 mw = g == 0 ? L_MX + l15 * 16 + w * 4 : L_MX + 256, pw = L_P + w * 1024 + l16;
    asm volatile("" : "+v"(ab), "+v"(ab1), "+v"(bb), "+v"(iw), "+v"(mw), "+v"(pw));
    asm volatile("" : "+s"(sqa));
    {
      u32 tg = 0;
      _Pragma("unroll 1") for (u32 it = 0; it < SPIN_CAP * 16; ++it) {
        tg = __builtin_amdgcn_readfirstlane(ldsld(L_CNT + 8));
        if (tg) break;
        __builtin_amdgcn_s_sleep(1);
      }
      _Pragma("unroll 1") for (u32 it = 0; it < 64; ++it)
        if ((int)((u32)__builtin_amdgcn_s_memrealtime() - tg) >= 0) break;
    }
    CLOB();
    STAMP(15);
#pragma unroll
    for (int k = 0; k < 4; ++k) pa[k] = ldc(rws, l16 + k * 1024, sqa);
    wait_kv(w + 1);
    STAMP(20);
#pragma unroll
    for (int k = 0; k < 16; ++k) af[0][k] = *lds<v4u>(ab + k * 16);
    _Pragma("unroll 1") for (u32 it = 0; it < SPIN_CAP; ++it) {
      u32 mm = 0;
#pragma unroll
      for (int k = 0; k < 4; ++k) {
        mm = max3u(max3u(mm, pa[k][0], pa[k][1]), pa[k][2], pa[k][3]);
        *lds<v4u>(iw + k * RS) = pa[k];
      }
      if (__all(mm != SENT)) break;
      __builtin_amdgcn_s_sleep(SL2);
      CLOB();
#pragma unroll
      for (int k = 0; k < 4; ++k) pa[k] = ldc(rws, l16 + k * 1024, sqa);
      st[21] += 1;
    }
    VM0();
    STAMP(5);
    BAR();
    STAMP(6);
    v4f s0 = Z4, s1 = Z4;
    {
      v4u bq[16];
#pragma unroll
      for (int s = 0; s < 16; ++s) bq[s] = *lds<v4u>(bb + s * 16);
      SCHED();
#pragma unroll
      for (int s = 0; s < 16; ++s) { af[1][s] = *lds<v4u>(ab1 + s * 16); s0 = mfma(af[0][s], bq[s], s0); SCHED(); }
#pragma unroll
      for (int s = 0; s < 16; ++s) s1 = mfma(af[1][s], bq[s], s1);
    }
    constexpr float SC = 0.08838834764831845f * 1.4426950408889634f;
    float mx = fmaxf(fmaxf(fmaxf(s0[0], s0[1]), fmaxf(s0[2], s0[3])), fmaxf(fmaxf(s1[0], s1[1]), fmaxf(s1[2], s1[3])));
    mx = __builtin_ceilf(mx32(mx16(mx)) * SC);
    *lds<float>(mw) = mx;
#define EX(v) __builtin_amdgcn_exp2f(fmaf(v, SC, -mx))
    *lds<v4u>(pw) = (v4u){pk2(EX(s0[0]), EX(s0[1])), pk2(EX(s0[2]), EX(s0[3])), pk2(EX(s1[0]), EX(s1[1])), pk2(EX(s1[2]), EX(s1[3]))};
    STAMP(17);
    BAR();
    STAMP(7);
    if (w == 0) {
      const v4f m4 = *lds<v4f>(L_MX + l15 * 16);
      const float M = fmaxf(fmaxf(m4[0], m4[1]), fmaxf(m4[2], m4[3]));
      float L = 0.f;
#pragma unroll
      for (int s = 0; s < 4; ++s) {
        const v4u pf = *lds<v4u>(L_P + s * 1024 + l16);
        float a = 0.f;
#pragma unroll
        for (int e = 0; e < 4; ++e) a = dot2(pf[e], 0x3F803F80u, a);
        L = fmaf(a, __builtin_amdgcn_exp2f(m4[s] - M), L);
      }
      L = tr16(L, L); L = tr32(L, L);
      if (g == 0) stc((v4u){f2u(M), tag, f2u(L), tag}, rws, l15 * 2048, WS_ML + type * 32768 + chunk * 16);
    }
    STAMP(8);
    __builtin_amdgcn_s_sleep(DLM);
  } else {
    const u32 v = w - 4, pg = L_CNT + 16 + 4 * v;
#define ROW0(b) ((b) < H0B ? (b) : (b) < H0B + AB ? (b) + 8 - AB - H0B : (b) - AB)
#define BLK0(b) do { if ((b) == 4) wait_ge(L_CNT, 8u); kv_blk<W0, b>(ckv, vkv + ROW0(b) * 16384, lkv); if ((b) + 1 > (W0 + 3) / 4) ldsst(pg, (b) + 1 - (W0 + 3) / 4); } while (0)
#define BLK1(b) do { kv_blk<W1, b>(ckv, vkv + (b) * 16384, lkv); if ((b) + 1 > (W1 + 3) / 4) ldsst(pg, (b) + 1 - (W1 + 3) / 4); } while (0)
    if (type == 0) {
      wait_ge(L_CNT + 4, 1u);
      STAMP(4);
      BLK0(0); BLK0(1); STAMP(23); BLK0(2); BLK0(3); STAMP(24); BLK0(4); BLK0(5); STAMP(25); BLK0(6); BLK0(7);
    } else {
      __builtin_amdgcn_s_sleep(DS1);
      STAMP(4);
      kv_blk<W1, 0, N0, 4>(ckv, vkv, lkv);
      BLK1(1); STAMP(23); BLK1(2); BLK1(3); STAMP(24); BLK1(4); BLK1(5); STAMP(25); BLK1(6); BLK1(7);
    }
    STAMP(3);
    v2u tl[4][8], th[4][8];
    u32 tb = L_KV + (4 * g + q4) * RS + v * 256 + (pp & 1) * 8 + (pp >> 1) * 128, tb1 = tb + 64 * RS;
    asm volatile("" : "+v"(tb), "+v"(tb1));
    wait_kv(4);
#pragma unroll
    for (int s = 0; s < 4; ++s)
#pragma unroll
      for (int t = 0; t < 8; ++t) tl[s][t] = trh(tb + 16 * s * RS + 16 * t);
    asm volatile("s_waitcnt vmcnt(8)" ::: "memory");
    ldsst(pg, 6u);
    wait_kv(6);
#pragma unroll
    for (int s = 0; s < 2; ++s)
#pragma unroll
      for (int t = 0; t < 8; ++t) th[s][t] = trh(tb1 + 16 * s * RS + 16 * t);
    u32 vu = (64 * j + 32 * (v & 1) + (v >> 1) + 8 * g) * 8192 + h * 256 + swz, v1 = (80 * j + 8 * v + (l >> 3)) * 3456 + h * 128 + (l & 7) * 16;
    u32 vo = g * 524288 + (l15 >> 3) * 16384 + (l & 7) * 16, so = WS_OP + (type * 128 + 2 * v) * 16384 + chunk * 128;
    u32 mr = L_MX + l15 * 16, pr = L_P + l16;
    asm volatile("" : "+v"(vu), "+v"(v1), "+v"(vo), "+v"(mr), "+v"(pr));
    asm volatile("" : "+s"(so));
    VM0();
    STAMP(5);
    BAR();
    STAMP(6);
    dma_run<4, 2 * 8192, 4096>(wuv, vu, L_UV + v * 1024);
    if (h < 27) {
      if (v < 2) dma_run<3, 32 * 3456, 4096>(w1, v1, L_W1 + v * 1024); else dma_run<2, 32 * 3456, 4096>(w1, v1, L_W1 + v * 1024);
    }
    __builtin_amdgcn_s_sleep(DPV);
#pragma unroll
    for (int s = 2; s < 4; ++s)
#pragma unroll
      for (int t = 0; t < 8; ++t) th[s][t] = trh(tb1 + 16 * s * RS + 16 * t);
    STAMP(17);
    BAR();
    STAMP(7);
    const v4f m4 = *lds<v4f>(mr);
    v4u pf[4];
#pragma unroll
    for (int s = 0; s < 4; ++s) pf[s] = *lds<v4u>(pr + s * 1024);
    const float M = fmaxf(fmaxf(m4[0], m4[1]), fmaxf(m4[2], m4[3]));
    v4f o[8];
#pragma unroll
    for (int s = 0; s < 4; ++s) {
      const u32 dl = (u32)fminf(M - m4[s], 255.f) * 0x800080u;
      const v4u ps = {pksub(pf[s][0], dl), pksub(pf[s][1], dl), pksub(pf[s][2], dl), pksub(pf[s][3], dl)};
#pragma unroll
      for (int t = 0; t < 8; ++t) o[t] = mfma(ps, (v4u){tl[s][t][0], tl[s][t][1], th[s][t][0], th[s][t][1]}, s ? o[t] : Z4);
    }
#pragma unroll
    for (int e = 0; e < 4; ++e)
      stc((v4u){pk2(o[0][e], o[1][e]), pk2(o[2][e], o[3][e]), pk2(o[4][e], o[5][e]), pk2(o[6][e], o[7][e])}, rws, vo, so + e * 131072);
    STAMP(8);
  }

  const u32 rcx = WS_R + type * R_SZ + (h & 15) * 8192;
  v4u a2[2], ow[4];
  auto late = [&]() {
    const rsrc_t ro = mk(wo, w < 5 ? 4096 * 640 * 2 : 0);
#pragma unroll
    for (int t = 0; t < 4; ++t) ow[t] = ldw(ro, (4 * g + t) * 1280 + l15 * 16, (h * 128 + j * 16) * 1280 + w * 256);
  };
  if (OW_EARLY) late();
  float rl;
  v4u uf[2];
  {
    v4u op[2], ml[2];
    _Pragma("unroll 1") for (u32 it = 0; it < SPIN_CAP; ++it) {
      CLOB();
#pragma unroll
      for (int t = 0; t < 2; ++t) ml[t] = ldc(rws, (l >> 3) * 16 + t * 128, WS_ML + h * 2048 + w * 256);
      if (__all(((ml[0][1] ^ tag) | (ml[0][3] ^ tag) | (ml[1][1] ^ tag) | (ml[1][3] ^ tag)) == 0)) break;
      __builtin_amdgcn_s_sleep(SL3);
    }
    STAMP(16);
    __builtin_amdgcn_s_sleep(DL3);
    CLOB();
#pragma unroll
    for (int t = 0; t < 2; ++t) op[t] = ldc(rws, l * 16 + t * 1024, WS_OP + (h * 8 + j) * 16384 + w * 2048);
    SCHED();
    const float m0 = u2f(ml[0][0]), m1 = u2f(ml[1][0]);
    float M = fmaxf(m0, m1);
    M = fmaxf(M, dpp<DPP_ROR8>(M)); M = mx32(mx16(M));
    float L = __builtin_amdgcn_exp2f(m0 - M) * u2f(ml[0][2]) + __builtin_amdgcn_exp2f(m1 - M) * u2f(ml[1][2]);
    L += dpp<DPP_ROR8>(L); L = tr16(L, L); L = tr32(L, L);
    if (l == 0) *lds<v2f>(L_ML + w * 8) = (v2f){M, L};
    BAR();
    STAMP(9);
    if (!OW_EARLY) late();
    const rsrc_t r2 = mk(w2, h < 27 && w < 5 ? 1728 * 640 * 2 : 0);
#pragma unroll
    for (int t = 0; t < 2; ++t) a2[t] = ldw(r2, (2 * g + t) * 1280 + l15 * 16, (64 * h + 8 * j) * 1280 + w * 256);
    SCHED();
    const v2f mi = *lds<v2f>(L_ML + (l & 7) * 8);
#pragma unroll
    for (int s = 0; s < 2; ++s) uf[s] = trd(L_UV + trq + s * 8192, L_UV + trq + s * 8192 + 1024);
    M = mi[0];
    M = fmaxf(M, dpp<DPP_X1>(M)); M = fmaxf(M, dpp<DPP_X2>(M)); M = fmaxf(M, dpp<DPP_HMIR>(M));
    L = __builtin_amdgcn_exp2f(mi[0] - M) * mi[1];
    L += dpp<DPP_X1>(L); L += dpp<DPP_X2>(L); L += dpp<DPP_HMIR>(L);
    rl = __builtin_amdgcn_rcpf(L);
    u32 cp = pk2(__builtin_amdgcn_exp2f(m0 - M), __builtin_amdgcn_exp2f(m1 - M));
    {
      v4f d = Z4;
      if (w < 4 && h < 27) {
        const u32 ta = L_W1 + (8 * g + q4) * 128 + w * 32 + pp * 8;
#pragma unroll
        for (int s = 0; s < 3; ++s) {
          v4u xf = *lds<v4u>(L_X + j * 160 + s * 64 + g * 16);
          const u32 tt = (s == 2 && g >= 2) ? ta + 48 * 128 : ta + s * 32 * 128;
          if (s == 2 && g >= 2) xf = (v4u){0u, 0u, 0u, 0u};
          d = mfma(xf, trd(tt, tt + 512), d);
        }
      }
      const float hp = d[0];
      __builtin_amdgcn_raw_buffer_store_b32(f2u(hp), rws, threadIdx.x < 256 && l < 16 ? (l >> 3) * 1024 + (l & 7) * 4 : 0x40000000u, rcx + w * 2048 + j * 128 + 64, 16);
    }
    asm volatile("" : "+v"(cp), "+v"(rl), "+v"(uf[0]), "+v"(uf[1]));
    STAMP(18);
    if (!__all(max3u(max4u(op[0]), max4u(op[1]), 0u) != SENT)) {
      _Pragma("unroll 1") for (u32 it = 0; it < SPIN_CAP; ++it) {
        __builtin_amdgcn_s_sleep(SL3);
        CLOB();
#pragma unroll
        for (int t = 0; t < 2; ++t) op[t] = ldc(rws, l * 16 + t * 1024, WS_OP + (h * 8 + j) * 16384 + w * 2048);
        st[21] += 256;
        if (__all(max3u(max4u(op[0]), max4u(op[1]), 0u) != SENT)) break;
      }
    }
    STAMP(10);
    float a[8];
#pragma unroll
    for (int e = 0; e < 4; ++e) {
      const u32 u0 = op[0][e], u1 = op[1][e];
      a[2 * e] = dot2(__builtin_amdgcn_perm(u1, u0, 0x05040100u), cp, 0.f);
      a[2 * e + 1] = dot2(__builtin_amdgcn_perm(u1, u0, 0x07060302u), cp, 0.f);
    }
    float o0, o1;
    red8(a, o0, o1);
    *lds<u32>(L_OA + (2 * w + ((l >> 3) & 1)) * 144 + ((l & 7) * 8 + g * 2) * 2) = pk2(o0, o1);
  }
  BAR();
  STAMP(19);
  {
    v4f c = mfma(*lds<v4u>(L_OA + l15 * 144 + g * 16), uf[0], Z4);
    c = mfma(*lds<v4u>(L_OA + l15 * 144 + 64 + g * 16), uf[1], c);
    float cs = (c[0] + c[1]) + (c[2] + c[3]);
    cs = tr16(cs, cs); cs = tr32(cs, cs);
    if (l < 16) __builtin_amdgcn_raw_buffer_store_b32(f2u(cs * rl), rws, l * 4, rcx + w * 1024 + j * 128, 16);
  }
  STAMP(11);
  if (w < 5) {
    __builtin_amdgcn_s_sleep(DL4);
    v4u cx; v2u hx;
    _Pragma("unroll 1") for (u32 it = 0; it < SPIN_CAP; ++it) {
      CLOB();
      cx = ldc(rws, (l & 7) * 128 + g * 16, rcx + j * 1024);
      hx = ldc2(rws, (l & 7) * 128 + g * 8 + 64, rcx + j * 1024);
      if (__all(max3u(max4u(cx), hx[0], hx[1]) != SENT)) break;
      __builtin_amdgcn_s_sleep(SL4);
    }
    STAMP(12);
    float a[8] = {0.f, 0.f, 0.f, 0.f, 0.f, 0.f, 0.f, 0.f};
#pragma unroll
    for (int t = 0; t < 4; ++t) {
      float c = u2f(cx[t]);
      c += dpp<DPP_X1>(c); c += dpp<DPP_X2>(c); c += dpp<DPP_HMIR>(c);
      fma8(a, ow[t], c);
    }
    float b[8] = {0.f, 0.f, 0.f, 0.f, 0.f, 0.f, 0.f, 0.f};
#pragma unroll
    for (int t = 0; t < 2; ++t) {
      float c = u2f(hx[t]);
      c += dpp<DPP_X1>(c); c += dpp<DPP_X2>(c); c += dpp<DPP_HMIR>(c);
      fma8(b, a2[t], fmaxf(c, 0.f));
    }
    float s0, s1, y0, y1;
    red8(a, s0, s1);
    red8(b, y0, y1);
    const u32 sp = pk2(s0, s1), yp = pk2(y0, y1);
    auto xs = __builtin_amdgcn_permlane16_swap(sp, sp, false, false);
    auto xy = __builtin_amdgcn_permlane16_swap(yp, yp, false, false);
    const u32 se = xs[0], so = xs[1], ye = xy[0], yo = xy[1];
    if ((g & 1) == 0) stc((v4u){se, ye, so, yo}, rws, l15 * 4096 + (g >> 1) * 16, WS_R + type * R_SZ + R_F + w * 16 * 4096 + chunk * 32);
    ldsst(L_CNT + 12, 1u);
  } else if (w == 5 && bi < 80) {
    const u32 me = 8 * bi + 4 * (l >> 5) + (l & 1) + ((l >> 1) & 2);
    const u32 xv = ((const u16*)x)[me];
    const u32 one = (l15 < 8) ? (0x3F80u << (16 * (l & 1))) : 0u;
    const v4u sel = {(l15 >> 1) == 0 ? one : 0u, (l15 >> 1) == 1 ? one : 0u, (l15 >> 1) == 2 ? one : 0u, (l15 >> 1) == 3 ? one : 0u};
    _Pragma("unroll 1") for (u32 it = 0; it < SPIN_CAP; ++it) {
      CLOB();
      const v2u t0 = ldc2(rws, l * 16, WS_ML + (h ^ 16) * 2048), t1 = ldc2(rws, l * 16 + 1024, WS_ML + (h ^ 16) * 2048);
      if (__all(t0[1] == tag && t1[1] == tag)) break;
      __builtin_amdgcn_s_sleep(SL5);
    }
    wait_ge(L_CNT + 12, 1u);
    __builtin_amdgcn_s_sleep(DL5);
    v4u f[8];
    _Pragma("unroll 1") for (u32 it = 0; it < SPIN_CAP; ++it) {
      CLOB();
      u32 mm = 0;
#pragma unroll
      for (int t = 0; t < 8; ++t) f[t] = ldc(rws, l * 16 + (t & 3) * 1024, WS_R + (t >> 2) * R_SZ + R_F + bi * 4096);
#pragma unroll
      for (int t = 0; t < 8; ++t) mm = max3u(max3u(mm, f[t][0], f[t][1]), f[t][2], f[t][3]);
      if (__all(mm != SENT)) break;
      __builtin_amdgcn_s_sleep(SL5);
      st[21] += 65536;
    }
    STAMP(12);
    v4f d0 = Z4, d1 = Z4;
#pragma unroll
    for (int t = 0; t < 4; ++t) { d0 = mfma(f[2 * t], sel, d0); d1 = mfma(f[2 * t + 1], sel, d1); }
    d0 += d1;
    float v = tr32(d0[0] + d0[2], d0[1] + d0[3]);
    v = rbf(tr16(v, v));
    const float yv = dpp<0xEE>(v);
    const float gt = rbf(yv * __builtin_amdgcn_rcpf(1.f + __builtin_amdgcn_exp2f(-1.4426950408889634f * yv)));
    const u32 ob = pk2(lo2f(xv) + rbf(v * gt), 0.f);
    if ((l & 0x1A) == 0) ((u16*)out)[me] = (u16)ob;
    if (bi == 0 && l == 0) __builtin_amdgcn_raw_buffer_store_b32(tag, rws, 0, WS_EPOCH, 16);
  }
  if (l == 0) {
    u32* sp = (u32*)(ws + WS_STAMP) + ((pi * 16 + type * 8 + xc) * 8 + w) * NST;
    st[NST - 1] = (__builtin_amdgcn_s_getreg(63508) & 15) | (((u32)(__builtin_amdgcn_s_memtime() - clk0)) << 4);
    st[NST - 2] = (u32)__builtin_amdgcn_s_memrealtime();
#pragma unroll
    for (int k = 0; k < NST; ++k) sp[k] = st[k];
  }
}

#define NWV 2048

__global__ void report_k(const char* ws) {
  const u32* st = (const u32*)(ws + WS_STAMP);
  const int t = threadIdx.x;
  const u32 beg = (u32)__builtin_amdgcn_s_memrealtime();
  u32 t0 = 0xFFFFFFFFu;
  for (int i = t; i < NWV; i += 64) t0 = min(t0, st[i * NST]);
  for (int o = 32; o; o >>= 1) t0 = min(t0, (u32)__shfl_xor((int)t0, o));
  u32 mn = 0xFFFFFFFFu, mx = 0, sm = 0, ct = 0;
  for (int i = t; i < NWV; i += 64) { u32 v = st[i * NST + (30)]; if (v) { v -= t0; mn = min(mn, v); mx = max(mx, v); sm += v; ct++; } }
  for (int o = 32; o; o >>= 1) { mn = min(mn, (u32)__shfl_xor((int)mn, o)); mx = max(mx, (u32)__shfl_xor((int)mx, o)); sm += __shfl_xor((int)sm, o); ct += __shfl_xor((int)ct, o); }
  const u32 av = ct ? sm / ct : 0;
  u32 q = (0);
  if (q > 5000) q = 5000;
  const u32 dur = 10000 + 20 * q;
  while ((u32)__builtin_amdgcn_s_memrealtime() - beg < dur) __builtin_amdgcn_s_sleep(1);
}

extern "C" void kernel_launch(void* const* d_in, const int* in_sizes, int n_in, void* d_out, int out_size, void* d_ws, size_t ws_size,
                              hipStream_t stream) {
  (void)in_sizes; (void)n_in; (void)out_size; (void)ws_size;
  (void)hipFuncSetAttribute((const void*)mla_ffn_kernel, hipFuncAttributeMaxDynamicSharedMemorySize, L_TOTAL);
  mla_ffn_kernel<<<256, 512, L_TOTAL, stream>>>((const u16*)d_in[0], (const u16*)d_in[1], (const u16*)d_in[2], (const u16*)d_in[3],
                                                (const u16*)d_in[4], (const u16*)d_in[5], (const u16*)d_in[7], (const u16*)d_in[8],
                                                (u16*)d_out, (char*)d_ws);
  report_k<<<1, 64, 0, stream>>>((const char*)d_ws);
}
```

```cpp
#define UKPOS 1
#define DL1 1
#define LST 1
#include <hip/hip_runtime.h>
#include <hip/hip_bf16.h>
#include <stdint.h>

typedef unsigned short u16;
typedef unsigned int u32;
typedef short v4s __attribute__((ext_vector_type(4)));
typedef __bf16 v8bf __attribute__((ext_vector_type(8)));
typedef __bf16 v2bf __attribute__((ext_vector_type(2)));
typedef float v4f __attribute__((ext_vector_type(4)));
typedef float v2f __attribute__((ext_vector_type(2)));
typedef u32 v4u __attribute__((ext_vector_type(4)));
typedef u32 v2u __attribute__((ext_vector_type(2)));
#define LAS __attribute__((address_space(3)))
typedef __amdgpu_buffer_rsrc_t rsrc_t;

#define W_AUX 0
#define KVNT 0
#define KV_NT ""
#define WNT 0
#define W_NT ""
#define N0 0
#define DS1 0
#define W1 8
#define W0 8
#define H0B 2
#define AB 1
#define DC1 60
#define DT 160
#define DPV 4
#define PFQ 2
#define PFK 8
#define DPK 0
#define WARM 0
#define PFC 1
#define OW_EARLY 0
#define NOCLR 0
#define HROT 0
#define SL1 1
#define DLG1 1
#define SLG 6
#define SL2 4
#define DLM 10
#define SL3 2
#define DL3 0
#define DL4 6
#define SL4 1
#define SL5 2
#define DL5 10

#define DSG 0
#define DO 200
#define SLS 4
#define NPF 16
#define EPD 4
#define UVE 1
#define PRIO 2
#define WPRE 0
#define TP 60
#define DF 165
#define TF 20
#define TH 0
#define DW 8
#define DX 170
#define DG 505
constexpr u32 WS_EPOCH = 0;
constexpr u32 WS_HINT = 4096;
constexpr u32 WS_Q1 = WS_HINT + 8192;
constexpr u32 WS_QA = WS_Q1 + 65536;
constexpr u32 WS_ML = WS_QA + 32768;
constexpr u32 WS_CX = WS_ML + 65536;
constexpr u32 WS_SIG = WS_CX + 368 * 1024;
constexpr u32 WS_F = WS_SIG + 32768;
constexpr u32 WS_OP = WS_F + 48 * 2560;
constexpr u32 WS_END = WS_OP + 256 * 16384;
constexpr u32 WS_STAMP = WS_END;

constexpr u32 RS = 1040;
constexpr u32 L_KV = 0;
constexpr u32 L_WK = 64 * RS;
constexpr u32 L_WQ = L_WK + 32 * 1088;
constexpr u32 L_UV = 0, L_W1 = 16384;
constexpr u32 L_QA = 128 * RS;
constexpr u32 L_P = L_QA + 16 * RS;
constexpr u32 L_X = L_P + 4096;
constexpr u32 L_MX = L_X + 1344;
constexpr u32 L_OA = L_MX + 272;
constexpr u32 L_ML = L_OA + 16 * 144;
constexpr u32 L_CNT = L_ML + 64;
constexpr u32 L_TOTAL = L_CNT + 96;
static_assert(L_WQ + 40960 <= L_P && L_W1 + 10240 <= 32 * RS, "staging");
static_assert(L_TOTAL <= 163840, "lds");

constexpr u32 SENT = 0xFFFFFFFFu;
constexpr u32 SPIN_CAP = 1u << 18;

__device__ __forceinline__ float u2f(u32 u) { return __uint_as_float(u); }
__device__ __forceinline__ u32 f2u(float f) { return __float_as_uint(f); }
__device__ __forceinline__ float lo2f(u32 u) { return __uint_as_float(u << 16); }
__device__ __forceinline__ float hi2f(u32 u) { return __uint_as_float(u & 0xFFFF0000u); }
__device__ __forceinline__ u32 pk2(float a, float b) { v2f f = {a, b}; v2bf c = __builtin_convertvector(f, v2bf); return *(u32*)&c; }
constexpr u32 LSB = 0x00010001u;
__device__ __forceinline__ u32 pkt(float a, float b, u32 mark) { return (__builtin_amdgcn_perm(f2u(b), f2u(a), 0x07060302u) & ~LSB) | mark; }
__device__ __forceinline__ u32 and4(v4u v) { return (v[0] & v[1]) & (v[2] & v[3]); }
__device__ __forceinline__ float rbf(float a) { return lo2f(pk2(a, a)); }
__device__ __forceinline__ u32 canon(u32 u) { return u < 0xFFFFFFFEu ? u : 0xFFFFFFFEu; }
__device__ __forceinline__ u32 max3u(u32 a, u32 b, u32 c) { u32 m = a > b ? a : b; return m > c ? m : c; }
__device__ __forceinline__ u32 max4u(v4u v) { return max3u(max3u(v[0], v[1], v[2]), v[3], v[3]); }
template <int CTRL> __device__ __forceinline__ float dpp(float v) { return u2f(__builtin_amdgcn_mov_dpp(f2u(v), CTRL, 0xF, 0xF, true)); }
constexpr int DPP_X1 = 0xB1, DPP_X2 = 0x4E, DPP_ROR4 = 0x124, DPP_ROR8 = 0x128, DPP_HMIR = 0x141;
__device__ __forceinline__ float fmx(float a, float b) { return __builtin_elementwise_maximum(a, b); }
template <int CTRL> __device__ __forceinline__ int dppi(int v) { return __builtin_amdgcn_mov_dpp(v, CTRL, 0xF, 0xF, true); }
__device__ __forceinline__ int imax8(int v) { v = max(v, dppi<0xB1>(v)); v = max(v, dppi<0x4E>(v)); return max(v, dppi<0x141>(v)); }
__device__ __forceinline__ u32 rtc() { return (u32)__builtin_amdgcn_s_memrealtime(); }
__device__ __forceinline__ void until(u32 t) {
  _Pragma("unroll 1") for (u32 it = 0; it < 256; ++it)
    if ((int)(rtc() - t) >= 0) break;
  asm volatile("" ::: "memory");
}
__device__ __forceinline__ float sum16(float v) { v += dpp<DPP_X1>(v); v += dpp<DPP_X2>(v); v += dpp<DPP_ROR4>(v); v += dpp<DPP_ROR8>(v); return v; }
__device__ __forceinline__ float max16(float v) { v = fmx(v, dpp<DPP_X1>(v)); v = fmx(v, dpp<DPP_X2>(v)); v = fmx(v, dpp<DPP_ROR4>(v)); v = fmx(v, dpp<DPP_ROR8>(v)); return v; }
__device__ __forceinline__ float tr16(float a, float b) { auto r = __builtin_amdgcn_permlane16_swap(f2u(a), f2u(b), false, false); u32 x = r[0], y = r[1]; return u2f(x) + u2f(y); }
__device__ __forceinline__ float tr32(float a, float b) { auto r = __builtin_amdgcn_permlane32_swap(f2u(a), f2u(b), false, false); u32 x = r[0], y = r[1]; return u2f(x) + u2f(y); }
__device__ __forceinline__ float mx16(float a) { auto r = __builtin_amdgcn_permlane16_swap(f2u(a), f2u(a), false, false); u32 x = r[0], y = r[1]; return fmx(u2f(x), u2f(y)); }
__device__ __forceinline__ float mx32(float a) { auto r = __builtin_amdgcn_permlane32_swap(f2u(a), f2u(a), false, false); u32 x = r[0], y = r[1]; return fmx(u2f(x), u2f(y)); }
__device__ __forceinline__ float sum64(float v) { v = sum16(v); v = tr16(v, v); return tr32(v, v); }
__device__ __forceinline__ float max64(float v) { v = max16(v); v = mx16(v); return mx32(v); }
__device__ __forceinline__ float dot2(u32 a, u32 b, float c) { return __builtin_amdgcn_fdot2_f32_bf16(*(v2bf*)&a, *(v2bf*)&b, c, false); }
__device__ __forceinline__ v4f mfma(v4u a, v4u b, v4f c) { return __builtin_amdgcn_mfma_f32_16x16x32_bf16(*(v8bf*)&a, *(v8bf*)&b, c, 0, 0, 0); }
__device__ __forceinline__ rsrc_t mk(const void* p, u32 n) { return __builtin_amdgcn_make_buffer_rsrc((void*)p, 0, n, 0x00020000); }
__device__ __forceinline__ v4u ldw(rsrc_t r, u32 v, u32 s) { return __builtin_amdgcn_raw_buffer_load_b128(r, v, s, W_AUX); }
__device__ __forceinline__ v4u ldc(rsrc_t r, u32 v, u32 s) { return __builtin_amdgcn_raw_buffer_load_b128(r, v, s, 16); }
__device__ __forceinline__ v2u ldc2(rsrc_t r, u32 v, u32 s) { return __builtin_amdgcn_raw_buffer_load_b64(r, v, s, 16); }
__device__ __forceinline__ void stc(v4u d, rsrc_t r, u32 v, u32 s) {
  __builtin_amdgcn_raw_buffer_store_b128(d, r, v, s, 16);
  asm volatile("s_nop 1" :: "v"(d));
}
__device__ __forceinline__ void stc2(v2u d, rsrc_t r, u32 v, u32 s) { __builtin_amdgcn_raw_buffer_store_b64(d, r, v, s, 16); }
__device__ __forceinline__ u16 ldx(rsrc_t r, u32 elem) { return __builtin_amdgcn_raw_buffer_load_b16(r, elem * 2, 0, 0); }
#define BAR() asm volatile("s_waitcnt lgkmcnt(0)\n\ts_barrier" ::: "memory")
#define SCHED() __builtin_amdgcn_sched_barrier(0)
#define CLOB() asm volatile("" ::: "memory")
#define CVM0() __builtin_amdgcn_s_waitcnt(0x0F70)
#define VM0() asm volatile("s_waitcnt vmcnt(0)" ::: "memory")
template <typename T> __device__ __forceinline__ LAS T* lds(u32 off) { return (LAS T*)(uintptr_t)off; }
__device__ __forceinline__ void fma8(float* a, v4u w, float x) {
#pragma unroll
  for (int e = 0; e < 4; ++e) { a[2 * e] = fmaf(lo2f(w[e]), x, a[2 * e]); a[2 * e + 1] = fmaf(hi2f(w[e]), x, a[2 * e + 1]); }
}
__device__ __forceinline__ void red8(const float* a, float& o0, float& o1) {
  float t0 = tr32(a[0], a[4]), t1 = tr32(a[1], a[5]), t2 = tr32(a[2], a[6]), t3 = tr32(a[3], a[7]);
  o0 = tr16(t0, t2); o1 = tr16(t1, t3);
}

__device__ __forceinline__ u32 ldsld(u32 off) { return __hip_atomic_load(lds<u32>(off), __ATOMIC_RELAXED, __HIP_MEMORY_SCOPE_WORKGROUP); }
__device__ __forceinline__ void ldsst(u32 off, u32 v) { __hip_atomic_store(lds<u32>(off), v, __ATOMIC_RELAXED, __HIP_MEMORY_SCOPE_WORKGROUP); }
__device__ __forceinline__ void wait_ge(u32 off, u32 v) {
  _Pragma("unroll 1") for (u32 it = 0; it < SPIN_CAP * 16; ++it) {
    if ((u32)__builtin_amdgcn_readfirstlane(ldsld(off)) >= v) break;
  }
  CLOB();
}
__device__ __forceinline__ void wait_kv(u32 v) {
  _Pragma("unroll 1") for (u32 it = 0; it < SPIN_CAP * 16; ++it) {
    CLOB();
    const v4u a = *(volatile LAS v4u*)lds<v4u>(L_CNT + 16);
    const u32 m = min(min(a[0], a[1]), min(a[2], a[3]));
    if ((u32)__builtin_amdgcn_readfirstlane(m) >= v) break;
  }
  CLOB();
}
template <int W, int SLOT> __device__ __forceinline__ void kv_row(const char* src, u32 voff, u32 lb) {
  asm volatile("s_add_u32 m0, %2, %3\n\ts_waitcnt vmcnt(%4)\n\tglobal_load_lds_dwordx4 %0, %1 offset:%5" KV_NT
               :: "v"(voff), "s"(src), "s"(lb), "n"(SLOT * RS - (SLOT & 3) * 1024), "n"(W - 1), "n"((SLOT & 3) * 1024) : "memory", "scc");
}
template <int W, int B, int FROM = 0, int TO = 4> __device__ __forceinline__ void kv_blk(const char* src, u32 voff, u32 lb) {
  if (FROM <= 0 && TO > 0) kv_row<W, 16 * B>(src, voff, lb);
  if (FROM <= 1 && TO > 1) kv_row<W, 16 * B + 1>(src, voff, lb);
  if (FROM <= 2 && TO > 2) kv_row<W, 16 * B + 2>(src, voff, lb);
  if (FROM <= 3 && TO > 3) kv_row<W, 16 * B + 3>(src, voff, lb);
}
template <int N, int GS, int LS> __device__ __forceinline__ void dma_run(const char* src, u32 voff, u32 dst) {
  asm volatile("s_mov_b32 m0, %2\n\ts_nop 0\n\tglobal_load_lds_dwordx4 %0, %1" W_NT "\n\t"
               ".rept %3\n\ts_add_u32 m0, m0, %5\n\tv_add_u32 %0, %4, %0\n\tglobal_load_lds_dwordx4 %0, %1" W_NT "\n\t.endr"
               : "+v"(voff) : "s"(src), "s"(dst), "n"(N - 1), "n"(GS), "n"(LS) : "memory", "scc");
}
__device__ __forceinline__ void dma(const char* src, u32 voff, u32 dst) {
  asm volatile("s_mov_b32 m0, %2\n\ts_nop 0\n\tglobal_load_lds_dwordx4 %0, %1" :: "v"(voff), "s"(src), "s"(dst) : "memory");
}
__device__ __forceinline__ v4u trd(u32 a0, u32 a1) {
  v4s r0 = __builtin_amdgcn_ds_read_tr16_b64_v4i16(lds<v4s>(a0)), r1 = __builtin_amdgcn_ds_read_tr16_b64_v4i16(lds<v4s>(a1));
  v2u b0 = *(v2u*)&r0, b1 = *(v2u*)&r1;
  return (v4u){b0[0], b0[1], b1[0], b1[1]};
}
__device__ __forceinline__ void lds_inc(u32 off) {
  asm volatile("s_waitcnt lgkmcnt(0)\n\ts_mov_b64 exec, 1\n\tds_add_u32 %0, %1\n\ts_mov_b64 exec, -1" :: "v"(off), "v"(1u) : "memory");
}
__device__ __forceinline__ v2u trh(u32 a) { v4s r = __builtin_amdgcn_ds_read_tr16_b64_v4i16(lds<v4s>(a)); return *(v2u*)&r; }
typedef u32 v16u __attribute__((ext_vector_type(16)));
typedef unsigned long long u64;
constexpr u32 OOB = 0x40000000u;
typedef unsigned short v2h __attribute__((ext_vector_type(2)));
__device__ __forceinline__ u32 pksub(u32 a, u32 b) { v2h r = __builtin_elementwise_sub_sat(*(v2h*)&a, *(v2h*)&b); return *(u32*)&r; }
constexpr v4f Z4 = {0.f, 0.f, 0.f, 0.f};

#define STAMP(k) do {} while (0)

__global__ void __launch_bounds__(512, 2)
mla_ffn_kernel(const u16*, const u16*, const u16*, const u16*, const u16*, const u16*, const u16*, const u16*, u16*, char*) {
  extern __shared__ __attribute__((aligned(16))) char smem_[];
  STAMP(0);
  v16u ka; v4u kb;
  asm volatile("s_load_dwordx16 %0, %2, 0x0\n\ts_load_dwordx4 %1, %2, 0x40" : "=&s"(ka), "=&s"(kb) : "s"(__builtin_amdgcn_kernarg_segment_ptr()));
  const u32 bi = blockIdx.x, xc = bi & 7, r = bi >> 3;
#define TB 0
  const u32 type = (r >> TB) & 1, pi = ((r >> (TB + 1)) << TB) | (r & ((1 << TB) - 1));
  const u32 chunk = xc * 16 + pi;
#define MAPT 1
#define MAPQ 1
  const u32 h = MAPT ? (1 - type) * 16 + pi : (1 - type) * 16 + xc * 2 + (pi >> 3), j = MAPT ? xc : pi & 7;
  const u32 hq = MAPQ ? ((pi + HROT) & 15) + 16 * (xc & 1) : xc * 4 + (pi >> 2), p = MAPQ ? xc >> 1 : pi & 3;
  const u32 w = __builtin_amdgcn_readfirstlane(threadIdx.x >> 6), l = threadIdx.x & 63;
  const u32 l15 = l & 15, g = l >> 4, q4 = l15 >> 2, pp = l & 3;
  const v4u sv = {SENT, SENT, SENT, SENT};
  const u32 swz = (l15 ^ (2 * g + 4 * (w & 2))) * 16;
  u32 vq = (160 * p * 8192 + hq * 256) + (threadIdx.x >> 4) * 8192 + swz, dq = L_WQ + w * 1024;
  u32 l16 = l * 16, l4 = l * 4;
  asm volatile("" : "+v"(vq), "+v"(l16), "+v"(l4));
  asm volatile("s_waitcnt lgkmcnt(0)" : "+s"(ka), "+s"(kb), "+s"(dq));
#define KP(v, i) ((const char*)(((u64)v[2 * (i) + 1] << 32) | v[2 * (i)]))
  const char *x = KP(ka, 0), *ckv = KP(ka, 1), *wq = KP(ka, 2), *wuk = KP(ka, 3), *wuv = KP(ka, 4), *wo = KP(ka, 5), *w1 = KP(ka, 6), *w2 = KP(ka, 7);
  char *out = (char*)KP(kb, 0), *ws = (char*)KP(kb, 1);

  v4u xa; u32 xb;
  const bool lnw = w == 0;
  if (lnw) asm volatile("global_load_dwordx4 %0, %2, %4\n\tglobal_load_dword %1, %3, %4 offset:1024" : "=&v"(xa), "=&v"(xb) : "v"(l16), "v"(l4), "s"(x) : "memory");
  if (type == 0) dma_run<5, 32 * 8192, 8192>(wq, vq, dq);
  SCHED();
  const u32 trq = 2 * g * 1024 + q4 * 256 + (((2 * w + (pp >> 1)) ^ (2 * q4 + 8 * (g & 1))) * 16) + (pp & 1) * 8;
  const u32 vk = (128 * p + 32 * w + g) * 8192 + hq * 256 + (l15 ^ g) * 16, dk = L_WK + w * 8 * 1088;
  const u32 vkv = chunk * 131072 + (w & 3) * 4096 + l16, lkv = (w & 3) * 4 * RS;
  if (type) {
    if (w >= 4) kv_blk<W1, 0, 0, N0>(ckv, vkv, lkv);
    else {
      if (PFQ) dma_run<2 * PFQ, 8192, 0>(wq, (160 * p + 160 - 32 * PFQ + 2 * PFQ * pi) * 8192 + (xc & 1) * 4096 + w * 1024 + l16, lkv + L_QA);
      if (PFC && w > 0 && w * 16 + pi < 16 + NPF) {
        u64 pc;
        asm volatile("s_getpc_b64 %0" : "=s"(pc));
        dma((const char*)(pc & ~1023ull), (w * 16 - 16 + pi) * 1024 + l16, L_P);
      }
      if (PFK) { __builtin_amdgcn_s_sleep(DPK); dma_run<PFK, 8192, 0>(wuk, (128 * p + 128 - 16 * PFK + PFK * pi) * 8192 + (xc & 1) * 4096 + w * 1024 + l16, lkv + L_QA); }
    }
  }
  if (w == 2 && l < 24) *lds<u32>(L_CNT + l4) = 0u;
  u32 tag;
  asm volatile("s_load_dword %0, %1, 0x0" : "=s"(tag) : "s"(ws) : "memory");
  const rsrc_t rws = mk(ws, WS_END);
  if (type == 0 && w == 5) stc(sv, rws, l < 16 ? l16 : OOB, WS_QA + hq * 1024 + p * 256);
  if (WARM == 1 && type == 0 && w == 6) dma(ws, l < 32 ? WS_Q1 + hq * 2048 + p * 512 + l16 : WS_HINT + (l & 7) * 1024 + (hq * 4 + p) / 2 * 16, L_P);
  if (type) BAR();
  if (lnw) {
    if (2 * PFQ + PFK < 5 && type) VM0();
    asm volatile("s_waitcnt vmcnt(5)" : "+v"(xa), "+v"(xb) :: "memory");
    float f[10];
#pragma unroll
    for (int k = 0; k < 4; ++k) { const u32 t = xa[k]; f[2 * k] = lo2f(t); f[2 * k + 1] = hi2f(t); }
    f[8] = lo2f(xb); f[9] = hi2f(xb);
    float s = f[0];
#pragma unroll
    for (int k = 1; k < 10; ++k) s += f[k];
    const float mean = sum64(s) * (1.f / 640.f);
    float v = 0.f;
#pragma unroll
    for (int k = 0; k < 10; ++k) { const float a = f[k] - mean; v = fmaf(a, a, v); }
    const float mb = rbf(mean);
    u32 c[5];
#pragma unroll
    for (int k = 0; k < 5; ++k) c[k] = pk2(f[2 * k] - mb, f[2 * k + 1] - mb);
    v = rbf(sum64(v) * (1.f / 640.f));
    const float rs = __builtin_amdgcn_rcpf(rbf(__builtin_amdgcn_sqrtf(rbf(v + 1e-5f))));
#pragma unroll
    for (int k = 0; k < 5; ++k) c[k] = pk2(lo2f(c[k]) * rs, hi2f(c[k]) * rs);
    *lds<v4u>(L_X + l16) = (v4u){c[0], c[1], c[2], c[3]};
    *lds<u32>(L_X + 1024 + l4) = c[4];
  }
  u32 vl = l15 * 128 + g * 32, sl = WS_Q1 + hq * 2048, lk = L_WK + w * 8 * 1088 + q4 * 1088 + pp * 256 + (g ^ pp) * 16;
  u32 vsa = (l & 17) == 0 ? l15 * 2 + (l & 32) : OOB, soa = WS_QA + hq * 1024 + p * 256 + w * 64;
  u32 vg = l * 8, sg = WS_HINT + xc * 1024 + type * 512;
  asm volatile("" : "+v"(vl), "+v"(lk), "+v"(vsa), "+v"(vg));
  asm volatile("" : "+s"(sl), "+s"(soa), "+s"(sg));
  if (type == 0) {
    BAR();
    if (UKPOS == 0 && w < 4) dma_run<8, 4 * 8192, 1088>(wuk, vk, dk);
    asm volatile("" : "+s"(tag));
    v4u a[5], b[5];
#pragma unroll
    for (int s = 0; s < 5; ++s) b[s] = *lds<v4u>(L_X + p * 320 + s * 64 + g * 16);
    v4u qd = {0u, tag + 1u, 0u, tag + 1u};
    u32 ta = L_WQ + trq, vs = l15 == 0 ? g * 16 : OOB, so = WS_Q1 + hq * 2048 + p * 512 + w * 64;
    u32 vh = w == 5 && l < 8 ? l * 1024 : OOB, sh = WS_HINT + (hq * 4 + p) * 8;
    asm volatile("" : "+v"(qd), "+v"(ta), "+v"(vs), "+v"(vh), "+v"(b[0]), "+v"(b[1]), "+v"(b[2]), "+v"(b[3]), "+v"(b[4]));
    asm volatile("" : "+s"(so), "+s"(sh));
    if (WARM == 2) { stc((v4u){tag, tag, tag, tag}, rws, vs, so); stc2((v2u){tag, tag}, rws, vh, sh); }
    if (UKPOS == 0 && w < 4) asm volatile("s_waitcnt vmcnt(%0)" :: "n"(WARM == 2 ? 10 : 8) : "memory"); else asm volatile("s_waitcnt vmcnt(%0)" :: "n"(WARM == 2 ? 2 : 0) : "memory");
    BAR();
#pragma unroll
    for (int s = 0; s < 5; ++s) a[s] = trd(ta + s * 8192, ta + s * 8192 + 1024);
    SCHED();
    v4f d = Z4;
#pragma unroll
    for (int s = 0; s < 5; ++s) d = mfma(a[s], b[s], d);
    qd[0] = pk2(d[0], d[1]); qd[2] = pk2(d[2], d[3]);
    stc(qd, rws, vs, so);
    stc2((v2u){qd[1], (u32)__builtin_amdgcn_s_memrealtime()}, rws, vh, sh);
    if (w >= 4) lds_inc(L_CNT);
    STAMP(2);
  }
  asm volatile("" : "+s"(tag));
  tag += 1u;
  const u32 mark = 65536u >> ((tag & 1) * 16), kx = mark ^ LSB;

  const u32 t10 = tag & 1023u, idx = (j - 5) * 32 + h;
  const u32 rg = j < 5 ? h : idx / 5, cs = j < 5 ? j : idx - rg * 5;
  const bool has = j < 5 || rg * 8 + w < 108;
  u32 oar, mlr, vcs, scs, vt10, vml, sml, sop, vkx, oaw, mlw, vwo, swo, vhp, shp, vmi, vcx, scx, fw, sfs;
  auto setup = [&]() {
    oar = L_OA + l15 * 144 + g * 16; mlr = L_ML + g * 16; vcs = l < 16 ? l4 : OOB; scs = WS_CX + (h * 8 + w) * 1024 + j * 128; vt10 = t10;
    vml = (l >> 3) * 16; sml = WS_ML + h * 2048 + w * 256; sop = WS_OP + (h * 8 + j) * 16384 + w * 2048; vkx = kx;
    oaw = L_OA + (2 * w + ((l >> 3) & 1)) * 144 + ((l & 7) * 8 + g * 2) * 2; mlw = L_ML + w * 8; vmi = L_CNT + 64 + w * 4;
    vwo = has ? g * 5120 + l15 * 16 : OOB; swo = __builtin_amdgcn_readfirstlane((rg * 128 + w * 16) * 1280 + cs * 256);
    vhp = w < 4 && h < 27 ? vcs : OOB; shp = WS_CX + (256 + h * 4 + w) * 1024 + j * 128;
    vcx = (l & 7) * 128 + g * 16; scx = __builtin_amdgcn_readfirstlane(WS_CX + ((j < 5 ? 0 : 256) + rg * 8 + w) * 1024);
    fw = L_P + w * 512 + l15 * 32 + g * 8; sfs = __builtin_amdgcn_readfirstlane(WS_F + ((j < 5 ? 0 : 32) + rg) * 2560 + cs * 512 + w * 256);
    asm volatile("" : "+v"(vml), "+v"(vkx), "+v"(oaw), "+v"(mlw), "+v"(oar), "+v"(mlr), "+v"(vcs), "+v"(vt10), "+v"(vwo), "+v"(vhp), "+v"(vmi), "+v"(vcx), "+v"(fw));
    asm volatile("" : "+s"(sml), "+s"(sop), "+s"(scs), "+s"(swo), "+s"(shp), "+s"(scx), "+s"(sfs));
  };
  u32 tg = 0;
  if (w < 4) {
    v2u hv;
    if (type == 0) {
      if (UKPOS == 1) dma_run<8, 4 * 8192, 1088>(wuk, vk, dk);
      __builtin_amdgcn_s_sleep(DL1);
      v4u pq[2];
      CLOB();
#pragma unroll
      for (int k = 0; k < 2; ++k) pq[k] = ldc(rws, vl + k * 16, sl);
      if (!LST) ldsst(L_CNT + 4, 1u);
      asm volatile("s_waitcnt vmcnt(%0)" :: "n"(UKPOS ? 2 : WARM == 2 ? 6 : 4) : "memory");
      v4u kf[2][4];
#pragma unroll
      for (int s = 0; s < 4; ++s)
#pragma unroll
        for (int T = 0; T < 2; ++T) kf[T][s] = *lds<v4u>(lk + T * 4 * 1088 + s * 64);
      lds_inc(L_CNT);
      float e[2];
      _Pragma("unroll 1") for (u32 it = 0; it < SPIN_CAP; ++it) {
        const v4u qf = {pq[0][0], pq[0][2], pq[1][0], pq[1][2]};
#pragma unroll
        for (int T = 0; T < 2; ++T) {
          const v4f d0 = mfma(qf, kf[T][0], Z4), d1 = mfma(qf, kf[T][1], Z4), d2 = mfma(qf, kf[T][2], Z4), d3 = mfma(qf, kf[T][3], Z4);
          e[T] = (d0[0] + d1[1]) + (d2[2] + d3[3]);
        }
        if (__all(((pq[0][1] ^ tag) | (pq[0][3] ^ tag) | (pq[1][1] ^ tag) | (pq[1][3] ^ tag)) == 0)) break;
        __builtin_amdgcn_s_sleep(SL1);
        CLOB();
#pragma unroll
        for (int k = 0; k < 2; ++k) pq[k] = ldc(rws, vl + k * 16, sl);
      }
      STAMP(3);
      float v = tr32(e[0], e[1]);
      v = tr16(v, v);
      const u32 pk = pk2(v, dpp<DPP_X1>(v));
      __builtin_amdgcn_raw_buffer_store_b32(pk, rws, vsa, soa, 16);
      if (LST) ldsst(L_CNT + 4, 1u);
      STAMP(4);
    } else __builtin_amdgcn_s_sleep(DC1);
    if (w == 0) {
      v2u h1, h2;
      if (type) __builtin_amdgcn_s_sleep(DLG1);
      CLOB();
      hv = ldc2(rws, vg, sg);
      if (type) __builtin_amdgcn_s_sleep(SLG);
      CLOB();
      h1 = ldc2(rws, vg, sg);
      _Pragma("unroll 1") for (u32 it = 0; it < SPIN_CAP; ++it) {
        __builtin_amdgcn_s_sleep(SLG);
        CLOB();
        h2 = ldc2(rws, vg, sg);
        if (__all(hv[0] == tag)) break;
        __builtin_amdgcn_s_sleep(SLG);
        CLOB();
        hv = ldc2(rws, vg, sg);
        if (__all(h1[0] == tag)) { hv = h1; break; }
        __builtin_amdgcn_s_sleep(SLG);
        CLOB();
        h1 = ldc2(rws, vg, sg);
        if (__all(h2[0] == tag)) { hv = h2; break; }
      }
      const u32 now = (u32)__builtin_amdgcn_s_memrealtime();
      float age = (float)(int)(now - hv[1]);
      age = -max64(-age);
      ldsst(L_CNT + 8, (now + DT - (u32)(int)fminf(fmx(age, 0.f), (float)DT)) | 1u);
    }
    v4u af[2][16], pa[4];
    u32 sqa = WS_QA + type * 16384 + w * 4096;
    u32 ab = L_KV + (16 * w + l15) * RS + g * 256, ab1 = ab + 64 * RS, bb = L_QA + l15 * RS + g * 256, iw = L_QA + 4 * w * RS + l16;
    u32 mw = g == 0 ? L_MX + l15 * 16 + w * 4 : L_MX + 256, pw = L_P + w * 1024 + l16;
    asm volatile("" : "+v"(ab), "+v"(ab1), "+v"(bb), "+v"(iw), "+v"(mw), "+v"(pw));
    asm volatile("" : "+s"(sqa));
    setup();
    {
      _Pragma("unroll 1") for (u32 it = 0; it < SPIN_CAP * 16; ++it) {
        tg = __builtin_amdgcn_readfirstlane(ldsld(L_CNT + 8));
        if (tg) break;
        __builtin_amdgcn_s_sleep(1);
      }
      _Pragma("unroll 1") for (u32 it = 0; it < 64; ++it)
        if ((int)((u32)__builtin_amdgcn_s_memrealtime() - tg) >= 0) break;
    }
    CLOB();
    STAMP(17);
#pragma unroll
    for (int k = 0; k < 4; ++k) pa[k] = ldc(rws, l16 + k * 1024, sqa);
    wait_kv(w + 1);
#pragma unroll
    for (int k = 0; k < 16; ++k) af[0][k] = *lds<v4u>(ab + k * 16);
    if (UVE) lds_inc(L_CNT + 40);
    _Pragma("unroll 1") for (u32 it = 0; it < SPIN_CAP; ++it) {
      u32 mm = 0;
#pragma unroll
      for (int k = 0; k < 4; ++k) {
        mm = max3u(max3u(mm, pa[k][0], pa[k][1]), pa[k][2], pa[k][3]);
        *lds<v4u>(iw + k * RS) = pa[k];
      }
      if (__all(mm != SENT)) break;
      __builtin_amdgcn_s_sleep(SL2);
      CLOB();
#pragma unroll
      for (int k = 0; k < 4; ++k) pa[k] = ldc(rws, l16 + k * 1024, sqa);
    }
    VM0();
    STAMP(5);
    BAR();
    STAMP(6);
    v4f s0 = Z4, s1 = Z4;
    {
      v4u bq[16];
#pragma unroll
      for (int s = 0; s < 16; ++s) bq[s] = *lds<v4u>(bb + s * 16);
      SCHED();
#pragma unroll
      for (int s = 0; s < 16; ++s) { af[1][s] = *lds<v4u>(ab1 + s * 16); s0 = mfma(af[0][s], bq[s], s0); SCHED(); }
#pragma unroll
      for (int s = 0; s < 16; ++s) s1 = mfma(af[1][s], bq[s], s1);
    }
    constexpr float SC = 0.08838834764831845f * 1.4426950408889634f;
    float mx = fmx(fmx(fmx(s0[0], s0[1]), fmx(s0[2], s0[3])), fmx(fmx(s1[0], s1[1]), fmx(s1[2], s1[3])));
    mx = __builtin_ceilf(mx32(mx16(mx)) * SC);
    *lds<float>(mw) = mx;
#define EX(v) __builtin_amdgcn_exp2f(fmaf(v, SC, -mx))
    *lds<v4u>(pw) = (v4u){pk2(EX(s0[0]), EX(s0[1])), pk2(EX(s0[2]), EX(s0[3])), pk2(EX(s1[0]), EX(s1[1])), pk2(EX(s1[2]), EX(s1[3]))};
    BAR();
    STAMP(7);
    if (w == 0) {
      const v4f m4 = *lds<v4f>(L_MX + l15 * 16);
      const float M = fmx(fmx(m4[0], m4[1]), fmx(m4[2], m4[3]));
      float L = 0.f;
#pragma unroll
      for (int s = 0; s < 4; ++s) {
        const v4u pf = *lds<v4u>(L_P + s * 1024 + l16);
        float a = 0.f;
#pragma unroll
        for (int e = 0; e < 4; ++e) a = dot2(pf[e], 0x3F803F80u, a);
        L = fmaf(a, __builtin_amdgcn_exp2f(m4[s] - M), L);
      }
      L = tr16(L, L); L = tr32(L, L);
      if (g == 0) stc((v4u){f2u(M), tag, f2u(L), tag}, rws, l15 * 2048, WS_ML + type * 32768 + chunk * 16);
    } else if (w == 1) {
      __builtin_amdgcn_s_sleep(EPD);
      const v4u sga = ldc(rws, l16, WS_SIG + ((1 - type) * 16 + pi) * 1024);
      const int bad = imax8((sga[0] ^ tag) | (sga[2] ^ tag) ? 1 : 0);
      asm volatile("" :: "v"(bad) : "memory");
      const u32 now = rtc();
      const int lead = imax8(max(max((int)(sga[1] - now), (int)(sga[3] - now)), -DO));
      if ((l & 7) == 0) ldsst(L_CNT + 64 + (l >> 1), bad ? 2u : (now + DO + min(lead, 0)) | 1u);
    }
    STAMP(8);
  } else {
    const u32 v = w - 4, pg = L_CNT + 16 + 4 * v;
#define ROW0(b) ((b) < H0B ? (b) : (b) < H0B + AB ? (b) + 8 - AB - H0B : (b) - AB)
#define BLK0(b) do { if ((b) == 4) wait_ge(L_CNT, 8u); kv_blk<W0, b>(ckv, vkv + ROW0(b) * 16384, lkv); if ((b) + 1 > (W0 + 3) / 4) ldsst(pg, (b) + 1 - (W0 + 3) / 4); } while (0)
#define BLK1(b) do { kv_blk<W1, b>(ckv, vkv + (b) * 16384, lkv); if ((b) + 1 > (W1 + 3) / 4) ldsst(pg, (b) + 1 - (W1 + 3) / 4); } while (0)
    setup();
    if (type == 0) {
      wait_ge(L_CNT + 4, 1u);
      STAMP(4);
      BLK0(0); BLK0(1); BLK0(2); BLK0(3); BLK0(4); BLK0(5); BLK0(6); BLK0(7);
    } else {
      __builtin_amdgcn_s_sleep(DS1);
      STAMP(4);
      kv_blk<W1, 0, N0, 4>(ckv, vkv, lkv);
      BLK1(1); BLK1(2); BLK1(3); BLK1(4); BLK1(5); BLK1(6); BLK1(7);
    }
    STAMP(3);
    v2u tl[4][8], th[4][8];
    u32 tb = L_KV + (4 * g + q4) * RS + v * 256 + (pp & 1) * 8 + (pp >> 1) * 128, tb1 = tb + 64 * RS;
    asm volatile("" : "+v"(tb), "+v"(tb1));
    wait_kv(4);
#pragma unroll
    for (int s = 0; s < 4; ++s)
#pragma unroll
      for (int t = 0; t < 8; ++t) tl[s][t] = trh(tb + 16 * s * RS + 16 * t);
    u32 vu = (64 * j + 32 * (v & 1) + (v >> 1) + 8 * g) * 8192 + h * 256 + swz, v1 = h < 27 ? (80 * j + 8 * v + (l >> 3)) * 3456 + h * 128 + (l & 7) * 16 : l16;
    u32 v1t = v < 2 ? v1 + 64 * 3456 : v1, d1t = L_W1 + v * 1024 + (v < 2 ? 8192 : 0);
    auto tiles_in = [&]() {
      dma_run<4, 2 * 8192, 4096>(wuv, vu, L_UV + v * 1024);
      dma_run<2, 32 * 3456, 4096>(w1, v1, L_W1 + v * 1024);
      dma(w1, v1t, d1t);
    };
    if (UVE) lds_inc(L_CNT + 40);
    asm volatile("s_waitcnt vmcnt(8)" ::: "memory");
    ldsst(pg, 6u);
    if (UVE == 1) { wait_ge(L_CNT + 40, 8u); tiles_in(); }
    STAMP(26);
    wait_kv(6);
#pragma unroll
    for (int s = 0; s < 2; ++s)
#pragma unroll
      for (int t = 0; t < 8; ++t) th[s][t] = trh(tb1 + 16 * s * RS + 16 * t);
    if (UVE == 2) { wait_ge(L_CNT + 40, 8u); tiles_in(); }
    u32 vo = g * 524288 + (l15 >> 3) * 16384 + (l & 7) * 16, so = WS_OP + (type * 128 + 2 * v) * 16384 + chunk * 128;
    u32 mr = L_MX + l15 * 16, pr = L_P + l16, vmark = mark;
    u32 vsg = v == 0 && l < 16 ? l * 128 * 8 : OOB, ssg = WS_SIG + (type * 16 * 128 + chunk) * 8, vtag = tag;
    asm volatile("" : "+v"(vu), "+v"(v1), "+v"(v1t), "+v"(vo), "+v"(mr), "+v"(pr), "+v"(vmark), "+v"(vsg), "+v"(vtag));
    asm volatile("" : "+s"(so), "+s"(ssg));
    asm volatile("s_waitcnt vmcnt(%0)" :: "n"(UVE ? 7 : 0) : "memory");
    STAMP(5);
    BAR();
    STAMP(6);
    stc2((v2u){vtag, (u32)__builtin_amdgcn_s_memrealtime()}, rws, vsg, ssg);
    if (!UVE) tiles_in();
    __builtin_amdgcn_s_sleep(DPV);
#pragma unroll
    for (int s = 2; s < 4; ++s)
#pragma unroll
      for (int t = 0; t < 8; ++t) th[s][t] = trh(tb1 + 16 * s * RS + 16 * t);
    __builtin_amdgcn_s_setprio(PRIO);
    BAR();
    STAMP(7);
    const v4f m4 = *lds<v4f>(mr);
    v4u pf[4];
#pragma unroll
    for (int s = 0; s < 4; ++s) pf[s] = *lds<v4u>(pr + s * 1024);
    const float M = fmx(fmx(m4[0], m4[1]), fmx(m4[2], m4[3]));
    v4f o[8];
#pragma unroll
    for (int s = 0; s < 4; ++s) {
      const u32 dl = (u32)fminf(M - m4[s], 255.f) * 0x800080u;
      const v4u ps = {pksub(pf[s][0], dl), pksub(pf[s][1], dl), pksub(pf[s][2], dl), pksub(pf[s][3], dl)};
#pragma unroll
      for (int t = 0; t < 8; ++t) o[t] = mfma(ps, (v4u){tl[s][t][0], tl[s][t][1], th[s][t][0], th[s][t][1]}, s ? o[t] : Z4);
    }
#pragma unroll
    for (int e = 0; e < 4; ++e)
      stc((v4u){pkt(o[0][e], o[1][e], vmark), pkt(o[2][e], o[3][e], vmark), pkt(o[4][e], o[5][e], vmark), pkt(o[6][e], o[7][e], vmark)}, rws, vo, so + e * 131072);
    __builtin_amdgcn_s_setprio(0);
    STAMP(8);
  }

  v4u ow[4], uf[2];
  {
    v4u op[2], ml[2];
    u32 hp;
    auto tiles = [&]() {
#pragma unroll
      for (int s = 0; s < 2; ++s) uf[s] = trd(L_UV + trq + s * 8192, L_UV + trq + s * 8192 + 1024);
      v4f d = Z4;
      if (w < 4 && h < 27) {
        const u32 ta = L_W1 + (8 * g + q4) * 128 + w * 32 + pp * 8;
#pragma unroll
        for (int s = 0; s < 3; ++s) {
          v4u xf = *lds<v4u>(L_X + j * 160 + s * 64 + g * 16);
          const u32 tt = (s == 2 && g >= 2) ? ta + 48 * 128 : ta + s * 32 * 128;
          if (s == 2 && g >= 2) xf = (v4u){0u, 0u, 0u, 0u};
          d = mfma(xf, trd(tt, tt + 512), d);
        }
      }
      hp = (f2u(d[0]) & ~1023u) | t10;
    };
    auto gather = [&]() {
      CLOB();
#pragma unroll
      for (int t = 0; t < 2; ++t) ml[t] = ldc(rws, vml + t * 128, sml);
#pragma unroll
      for (int t = 0; t < 2; ++t) op[t] = ldc(rws, l16 + t * 1024, sop);
      SCHED();
    };
    auto good = [&]() {
      op[0] ^= vkx; op[1] ^= vkx;
      return __all(((ml[0][1] ^ tag) | (ml[0][3] ^ tag) | (ml[1][1] ^ tag) | (ml[1][3] ^ tag) | (~(and4(op[0]) & and4(op[1])) & LSB)) == 0);
    };
    u32 mine = 0;
    if (w >= 4) { asm volatile("s_waitcnt vmcnt(%0)" :: "n"(UVE ? 5 : 4) : "memory"); lds_inc(L_CNT + 32); }
    _Pragma("unroll 1") for (u32 it = 0; it < SPIN_CAP * 16; ++it) {
      mine = __builtin_amdgcn_readfirstlane(ldsld(vmi));
      if (mine) break;
      __builtin_amdgcn_s_sleep(1);
    }
    STAMP(9);
    const bool first = (mine & 1) && (int)(rtc() + TF - mine) >= 0;
    if (!first) { wait_ge(L_CNT + 32, 4u); tiles(); }
    STAMP(18);
    if (!(mine & 1)) {
      const u32 vsg = l15 * 8, ssg = WS_SIG + (((1 - type) * 16 + pi) * 128 + 16 * w) * 8;
      v2u sg;
      _Pragma("unroll 1") for (u32 it = 0; it < SPIN_CAP; ++it) {
        CLOB();
        sg = ldc2(rws, vsg, ssg);
        if (__all(sg[0] == tag)) break;
        __builtin_amdgcn_s_sleep(SLS);
      }
      const u32 t = rtc();
      int lead = imax8(max((int)(sg[1] - t), -DO));
      lead = max(lead, dppi<DPP_ROR8>(lead));
      mine = __builtin_amdgcn_readfirstlane(t + DO + min(lead, 0));
    }
    until(mine);
    asm volatile("s_nop 4\n\tbuffer_load_dwordx4 %0, %4, %6, %7 offen sc1\n\tbuffer_load_dwordx4 %1, %4, %6, %7 offen offset:128 sc1\n\t"
                 "buffer_load_dwordx4 %2, %5, %6, %8 offen sc1\n\tbuffer_load_dwordx4 %3, %5, %6, %8 offen offset:1024 sc1"
                 : "=&v"(ml[0]), "=&v"(ml[1]), "=&v"(op[0]), "=&v"(op[1]) : "v"(vml), "v"(l16), "s"(rws), "s"(sml), "s"(sop) : "memory");
    if (DX) ldsst(vmi, mine);
    lds_inc(L_CNT + 36);
    STAMP(16);
    if (first) { wait_ge(L_CNT + 32, 4u); tiles(); }
    const rsrc_t ro = mk(j < 5 ? wo : w2, OOB);
    wait_ge(L_CNT + 36, 8u);
    __builtin_amdgcn_s_sleep(DW);
    STAMP(20);
    asm volatile("s_nop 4\n\tbuffer_store_dword %8, %9, %10, %11 offen sc1\n\t"
                 "buffer_load_dwordx4 %4, %12, %13, %14 offen\n\tbuffer_load_dwordx4 %5, %12, %13, %14 offen offset:1280\n\t"
                 "buffer_load_dwordx4 %6, %12, %13, %14 offen offset:2560\n\tbuffer_load_dwordx4 %7, %12, %13, %14 offen offset:3840\n\t"
                 "s_waitcnt vmcnt(5)"
                 : "+v"(op[0]), "+v"(op[1]), "+v"(ml[0]), "+v"(ml[1]), "=&v"(ow[0]), "=&v"(ow[1]), "=&v"(ow[2]), "=&v"(ow[3])
                 : "v"(hp), "v"(vhp), "s"(rws), "s"(shp), "v"(vwo), "s"(ro), "s"(swo) : "memory");
    const bool ok = good();
    if (!ok) {
      _Pragma("unroll 1") for (u32 it = 0; it < SPIN_CAP; ++it) {
        __builtin_amdgcn_s_sleep(SL3);
        gather();
        if (good()) break;
      }
    }
    STAMP(10);
    const float m0 = u2f(ml[0][0]), m1 = u2f(ml[1][0]);
    float M = fmx(m0, m1);
    M = fmx(M, dpp<DPP_ROR8>(M)); M = mx32(mx16(M));
    const float c0 = __builtin_amdgcn_exp2f(m0 - M), c1 = __builtin_amdgcn_exp2f(m1 - M);
    const u32 cp = pk2(c0, c1);
    float a[8];
#pragma unroll
    for (int e = 0; e < 4; ++e) {
      const u32 u0 = op[0][e], u1 = op[1][e];
      a[2 * e] = dot2(__builtin_amdgcn_perm(u1, u0, 0x05040100u), cp, 0.f);
      a[2 * e + 1] = dot2(__builtin_amdgcn_perm(u1, u0, 0x07060302u), cp, 0.f);
    }
    float o0, o1;
    red8(a, o0, o1);
    *lds<u32>(oaw) = pk2(o0, o1);
    float L = c0 * u2f(ml[0][2]) + c1 * u2f(ml[1][2]);
    L += dpp<DPP_ROR8>(L); L = tr16(L, L); L = tr32(L, L);
    *lds<v2f>(mlw) = (v2f){M, L};
  }
  BAR();
  const u32 t7 = rtc();
  {
    const v4f mi = *lds<v4f>(mlr);
    const v4u x0 = *lds<v4u>(oar), x1 = *lds<v4u>(oar + 64);
    SCHED();
    const v4f c = mfma(x1, uf[1], mfma(x0, uf[0], Z4));
    const float M = mx32(mx16(fmx(mi[0], mi[2])));
    const float f0 = __builtin_amdgcn_exp2f(mi[0] - M), f1 = __builtin_amdgcn_exp2f(mi[2] - M);
    float L = f0 * mi[1] + f1 * mi[3];
    L = tr16(L, L); L = tr32(L, L);
    const float rl = __builtin_amdgcn_rcpf(L);
    float cs = (c[0] + c[1]) * (f0 * rl) + (c[2] + c[3]) * (f1 * rl);
    cs = tr16(cs, cs); cs = tr32(cs, cs);
    __builtin_amdgcn_raw_buffer_store_b32((f2u(cs) & ~1023u) | vt10, rws, vcs, scs, 16);
  }
  STAMP(11);
  if (!has) asm volatile("s_waitcnt vmcnt(1)" : "+v"(ow[0]), "+v"(ow[1]), "+v"(ow[2]), "+v"(ow[3]) :: "memory");
  if (j < 5 || idx < 70) {
    float s0 = 0.f, s1 = 0.f;
    if (has) {
      if (DX) {
        const int rel = imax8((int)(ldsld(L_CNT + 64 + (l & 7) * 4) - t7));
        until(t7 + DX + __builtin_amdgcn_readfirstlane(rel));
      } else __builtin_amdgcn_s_sleep(DL4);
      v4u cx;
      _Pragma("unroll 1") for (u32 it = 0; it < SPIN_CAP; ++it) {
        CLOB();
        cx = ldc(rws, vcx, scx) ^ t10;
        if (__all(((cx[0] | cx[1] | cx[2] | cx[3]) & 1023u) == 0)) break;
        __builtin_amdgcn_s_sleep(SL4);
      }
      STAMP(12);
      asm volatile("" : "+v"(ow[0]), "+v"(ow[1]), "+v"(ow[2]), "+v"(ow[3]) : "v"(cx));
      const float lo = j < 5 ? -__builtin_inff() : 0.f;
      float a[8] = {0.f, 0.f, 0.f, 0.f, 0.f, 0.f, 0.f, 0.f};
#pragma unroll
      for (int t = 0; t < 4; ++t) {
        float c = u2f(cx[t]);
        c += dpp<DPP_X1>(c); c += dpp<DPP_X2>(c); c += dpp<DPP_HMIR>(c);
        fma8(a, ow[t], fmx(c, lo));
      }
      red8(a, s0, s1);
    }
    *lds<v2f>(fw) = (v2f){s0, s1};
    BAR();
    if (w < 2) {
      float r[8];
#pragma unroll
      for (int t = 0; t < 8; ++t) r[t] = *lds<float>(L_P + t * 512 + threadIdx.x * 4);
      const float f = ((r[0] + r[1]) + (r[2] + r[3])) + ((r[4] + r[5]) + (r[6] + r[7]));
      __builtin_amdgcn_raw_buffer_store_b32((f2u(f) & ~1023u) | vt10, rws, l4, sfs, 16);
      STAMP(14);
    }
  } else if (idx < 90 && w < 2) {
    const u32 c0 = (idx - 70) * 32 + w * 16, me = c0 + 4 * (l & 3) + g;
    const u32 xv = ((const u16*)x)[me];
    const u32 vf = (l >> 2) * 2560 + (l & 3) * 16, vy = l < 56 ? vf : OOB, ty = l < 56 ? t10 : 0u;
    if (DG) {
      const v4u a = ldc(rws, l16, WS_SIG + pi * 1024), b = ldc(rws, l16, WS_SIG + (16 + pi) * 1024);
      int lead = -DG;
#pragma unroll
      for (int e = 0; e < 4; e += 2) lead = max(lead, max(a[e] == tag ? (int)(a[e + 1] - t7) : -DG, b[e] == tag ? (int)(b[e + 1] - t7) : -DG));
      until(t7 + DG + __builtin_amdgcn_readfirstlane((int)max64((float)min(lead, 0))));
    } else until(t7 + DF);
    STAMP(13);
    v4u f[3];
    _Pragma("unroll 1") for (u32 it = 0; it < SPIN_CAP; ++it) {
      CLOB();
      f[0] = ldc(rws, vf, WS_F + c0 * 4); f[1] = ldc(rws, vf, WS_F + 16 * 2560 + c0 * 4); f[2] = ldc(rws, vy, WS_F + 32 * 2560 + c0 * 4);
      f[0] ^= t10; f[1] ^= t10; f[2] ^= ty;
      u32 bad = 0;
#pragma unroll
      for (int e = 0; e < 4; ++e) bad |= f[0][e] | f[1][e] | f[2][e];
      if (__all((bad & 1023u) == 0)) break;
      __builtin_amdgcn_s_sleep(SL5);
    }
    STAMP(15);
    float v = tr16(tr32(u2f(f[0][0]) + u2f(f[1][0]), u2f(f[0][2]) + u2f(f[1][2])), tr32(u2f(f[0][1]) + u2f(f[1][1]), u2f(f[0][3]) + u2f(f[1][3])));
    float yv = tr16(tr32(u2f(f[2][0]), u2f(f[2][2])), tr32(u2f(f[2][1]), u2f(f[2][3])));
    v += dpp<DPP_ROR4>(v); yv += dpp<DPP_ROR4>(yv); v += dpp<DPP_ROR8>(v); yv += dpp<DPP_ROR8>(yv);
    v = rbf(v); yv = rbf(yv);
    const float gt = rbf(yv * __builtin_amdgcn_rcpf(1.f + __builtin_amdgcn_exp2f(-1.4426950408889634f * yv)));
    const u32 ob = pk2(lo2f(xv) + rbf(v * gt), 0.f);
    if (l15 < 4) ((u16*)out)[me] = (u16)ob;
    if (c0 == 0 && l == 0) __builtin_amdgcn_raw_buffer_store_b32(tag, rws, 0, WS_EPOCH, 16);
  }
}

extern "C" void kernel_launch(void* const* d_in, const int* in_sizes, int n_in, void* d_out, int out_size, void* d_ws, size_t ws_size,
                              hipStream_t stream) {
  (void)in_sizes; (void)n_in; (void)out_size; (void)ws_size;
  (void)hipFuncSetAttribute((const void*)mla_ffn_kernel, hipFuncAttributeMaxDynamicSharedMemorySize, L_TOTAL);
  mla_ffn_kernel<<<256, 512, L_TOTAL, stream>>>((const u16*)d_in[0], (const u16*)d_in[1], (const u16*)d_in[2], (const u16*)d_in[3],
                                                (const u16*)d_in[4], (const u16*)d_in[5], (const u16*)d_in[7], (const u16*)d_in[8],
                                                (u16*)d_out, (char*)d_ws);
}
```
